```python
import torch

def _softmax(x, axis=-1):
    return torch.softmax(x, dim=axis)

def _gelu(x):
    return torch.nn.functional.gelu(x, approximate='tanh')

def _silu(x):
    return torch.nn.functional.silu(x)

def _sigmoid(x):
    return torch.sigmoid(x)

def _layernorm(x, axis=-1, eps=1e-05):
    m = x.mean(dim=axis, keepdim=True)
    v = x.var(dim=axis, keepdim=True, unbiased=False)
    return (x - m) / torch.sqrt(v + eps)

def _scatter_add(tgt, idx, src, axis=0):
    return tgt.clone().index_add_(axis, idx, src)

def _segment_sum(src, seg, n):
    out = torch.zeros((n,) + src.shape[1:], dtype=src.dtype)
    return out.index_add_(0, seg, src)

def _pad(x, pw):
    flat = tuple((v for pair in reversed(pw) for v in pair))
    return torch.nn.functional.pad(x, flat)
kernel_name = 'standalone__varlen_attn_packed__ragged'

def setup_inputs():

    _w = lambda a: (lambda t: t.to(torch.bfloat16) if t.is_floating_point() else t)(torch.as_tensor(a))
    cu = torch.tensor([0, 238, 376, 669, 835, 1320, 1545, 1865, 2058], dtype=torch.int64)
    T, H, D = (int(cu[-1]), 16, 192)
    return {'q': _w((lambda *s: torch.randn(*s, dtype=torch.bfloat16) * 0.8)(T, H, D)), 'k': _w((lambda *s: torch.randn(*s, dtype=torch.bfloat16) * 0.8)(T, H, D)), 'v': _w((lambda *s: torch.randn(*s, dtype=torch.bfloat16) * 0.05)(T, H, D)), 'cu': _w(cu)}

def reference(q, k, v, cu):
    T, H, D = q.shape
    out = torch.zeros_like(q)
    scale = D ** (-0.5)
    outs = []
    for i in range(len(cu) - 1):
        s, e = (int(cu[i]), int(cu[i + 1]))
        qi, ki, vi = (q[s:e].clone(), k[s:e].clone(), v[s:e].clone())
        p = _softmax(torch.einsum('qhd,khd->hqk', qi, ki) * scale, axis=-1)
        outs.append(torch.einsum('hqk,khd->qhd', p, vi))
    return torch.concatenate(outs, axis=0)
```

```cpp
#include <hip/hip_runtime.h>
#include <hip/hip_bf16.h>

typedef __attribute__((ext_vector_type(8))) short bf16x8;
typedef __attribute__((ext_vector_type(4))) short s16x4;
typedef __attribute__((ext_vector_type(16))) float f32x16;
typedef __attribute__((ext_vector_type(4))) float f32x4;
typedef __attribute__((ext_vector_type(2))) float f32x2;
typedef __attribute__((ext_vector_type(4))) unsigned u32x4;
typedef __attribute__((ext_vector_type(2))) unsigned u32x2;
typedef __attribute__((ext_vector_type(2))) __bf16 bf16x2;
#define LDS(T) __attribute__((address_space(3))) T
#define DI __device__ __forceinline__

constexpr int NH = 16, HD = 192;
constexpr unsigned ROWB = NH * HD * 2;
constexpr unsigned HEADB = HD * 2;
constexpr unsigned TILEB = 32 * HEADB;
constexpr unsigned SLOTB = 2 * TILEB;
constexpr unsigned HALFB = 3 * SLOTB;
constexpr unsigned ML_OFF = 2 * HALFB;
constexpr unsigned LDS_BYTES = ML_OFF + 8 * 512;
constexpr float CEXP = 0.07216878364870322f * 1.4426950408889634f;
constexpr float THR = 8.0f;

DI unsigned rfl(unsigned x) { return __builtin_amdgcn_readfirstlane(x); }
DI unsigned cvtpk(float lo, float hi) { f32x2 v = {lo, hi}; bf16x2 b = __builtin_convertvector(v, bf16x2); return __builtin_bit_cast(unsigned, b); }
DI float swap_max(float x) { auto r = __builtin_amdgcn_permlane32_swap(__float_as_uint(x), __float_as_uint(x), false, false); return __builtin_fmaxf(__uint_as_float(r[0]), __uint_as_float(r[1])); }
DI float swap_add(float x) { auto r = __builtin_amdgcn_permlane32_swap(__float_as_uint(x), __float_as_uint(x), false, false); return __uint_as_float(r[0]) + __uint_as_float(r[1]); }
DI __amdgpu_buffer_rsrc_t mk_rsrc(const void* base, unsigned long long off, unsigned bytes) {
  unsigned long long a = (unsigned long long)base + off;
  unsigned lo = rfl((unsigned)a), hi = rfl((unsigned)(a >> 32));
  return __builtin_amdgcn_make_buffer_rsrc((void*)(((unsigned long long)hi << 32) | lo), (short)0, (int)rfl(bytes), 0x00020000);
}
DI void dma3(__amdgpu_buffer_rsrc_t rs, unsigned voff, unsigned ldsdst) {
  unsigned keep;
  asm volatile("s_mov_b32 %0, m0\n\t"
               "s_mov_b32 m0, %3\n\ts_nop 0\n\t"
               "buffer_load_dwordx4 %1, %2, 0 offen lds\n\t"
               "s_add_u32 m0, m0, 3968\n\ts_nop 0\n\t"
               "buffer_load_dwordx4 %1, %2, 0 offen offset:128 lds\n\t"
               "s_add_u32 m0, m0, 3968\n\ts_nop 0\n\t"
               "buffer_load_dwordx4 %1, %2, 0 offen offset:256 lds\n\t"
               "s_mov_b32 m0, %0"
               : "=&s"(keep) : "v"(voff), "s"(rs), "s"(ldsdst) : "memory", "scc");
}
#define MFMA(a, b, c) __builtin_amdgcn_mfma_f32_32x32x16_bf16((a), (b), (c), 0, 0, 0)
#define MX3(a, b, c) __builtin_fmaxf(__builtin_fmaxf((a), (b)), (c))
typedef short v4i16_t __attribute__((ext_vector_type(4)));
DI s16x4 vtr(unsigned a) { return __builtin_bit_cast(s16x4, __builtin_amdgcn_ds_read_tr16_b64_v4i16((LDS(v4i16_t)*)a)); }

__global__ __launch_bounds__(512, 2) void varlen_attn_kernel(const __hip_bfloat16* __restrict__ Q, const __hip_bfloat16* __restrict__ K,
                                                            const __hip_bfloat16* __restrict__ V, const int* __restrict__ cu,
                                                            __hip_bfloat16* __restrict__ O, int nseq, int G) {
  extern __shared__ __attribute__((aligned(16))) char smem[];
  const unsigned lds0 = (unsigned)(size_t)smem;
  const int lane = threadIdx.x & 63;
  const int wid = rfl(threadIdx.x >> 6), half = wid >> 2, wq = wid & 3;
  const int b = blockIdx.x;
  const int head = 2 * (b & 7) + ((b >> 3) & 1), g = b >> 4;

  int c0 = 0, len = 0;
  if (lane < nseq) { c0 = cu[lane]; len = cu[lane + 1] - c0; }
  const int nb = (len + 127) >> 7;
  int start = 0, J = 0;
  for (int j = 0; j < nseq; ++j) {
    const int lj = __builtin_amdgcn_readlane(len, j), nj = (lj + 127) >> 7;
    start += (lj > len || (lj == len && j < lane)) ? nj : 0;
    J += nj;
  }
  const int S = min(J, 2 * G - J);
  const bool split = g < S;
  const int u = split ? g : (half == 0 ? g : J - 1 - (g - S));
  if (g >= J || (!split && half == 1 && u <= g)) return;
  const unsigned long long hit = __ballot(lane < nseq && start <= u && u < start + nb);
  const int si = __builtin_ctzll(hit);
  const int seq0 = __builtin_amdgcn_readlane(c0, si), L = __builtin_amdgcn_readlane(len, si);
  const int qrow = (u - __builtin_amdgcn_readlane(start, si)) << 7;
  const int nq = min(128, L - qrow);
  const int ntile = (L + 31) >> 5;
  const int n0 = (ntile + 1) >> 1;
  const int t0 = (split && half) ? n0 : 0;
  const int nt = split ? (half ? ntile - n0 : n0) : ntile;
  const int nsteps = split ? n0 : ntile;
  const int nkeys = min(L - t0 * 32, nt * 32);

  const unsigned long long kvoff = (unsigned long long)(seq0 + t0 * 32) * ROWB + head * HEADB;
  const unsigned kvbytes = nkeys > 0 ? (unsigned)(nkeys - 1) * ROWB + HEADB : 0u;
  const __amdgpu_buffer_rsrc_t rsK = mk_rsrc(K, kvoff, kvbytes), rsV = mk_rsrc(V, kvoff, kvbytes);
  const unsigned long long qoff = (unsigned long long)(seq0 + qrow) * ROWB + head * HEADB;
  const unsigned qbytes = (unsigned)(nq - 1) * ROWB + HEADB;
  const __amdgpu_buffer_rsrc_t rsQ = mk_rsrc(Q, qoff, qbytes);

  const int r32 = lane & 31, hi = lane >> 5;
  const unsigned ring = lds0 + half * HALFB;
  const int drow = 8 * wq + (lane >> 3), dk = lane & 7;
  unsigned voffK = drow * ROWB + 16 * (dk ^ ((drow >> 1) & 7));
  unsigned voffV = drow * ROWB + 16 * (dk ^ (((drow >> 1) & 1) << 2));
  const unsigned ddst = ring + wq * 1024;

  dma3(rsK, voffK, ddst);
  bf16x8 qf[12];
  {
    const unsigned vq = (32 * wq + r32) * ROWB + 16 * hi;
#define QLD(s) asm volatile("buffer_load_dwordx4 %0, %1, %2, 0 offen offset:%3" : "=v"(qf[s]) : "v"(vq), "s"(rsQ), "i"(32 * (s)) : "memory")
    QLD(0); QLD(1); QLD(2); QLD(3); QLD(4); QLD(5); QLD(6); QLD(7); QLD(8); QLD(9); QLD(10); QLD(11);
#undef QLD
  }
  dma3(rsV, voffV, ddst + TILEB);
  voffK += 32 * ROWB; voffV += 32 * ROWB;
  dma3(rsK, voffK, ddst + SLOTB); dma3(rsV, voffV, ddst + SLOTB + TILEB);
  voffK += 32 * ROWB; voffV += 32 * ROWB;

  const unsigned kb = r32 * 128 + 16 * (hi ^ ((r32 >> 1) & 7));
  const unsigned vb = TILEB + (4 * hi + ((lane >> 2) & 3)) * 128 + 64 * ((lane >> 3) & 1) + 32 * ((lane >> 4) & 1) + 8 * (lane & 3);

  f32x16 o[6];
#pragma unroll
  for (int d = 0; d < 6; ++d) o[d] = f32x16{};
  float mref = -1e30f, lsum = 0.f;
  const bool active = 32 * wq < nq;

  asm volatile("s_waitcnt vmcnt(6)" : "+v"(qf[0]), "+v"(qf[1]), "+v"(qf[2]), "+v"(qf[3]), "+v"(qf[4]), "+v"(qf[5]), "+v"(qf[6]), "+v"(qf[7]),
               "+v"(qf[8]), "+v"(qf[9]), "+v"(qf[10]), "+v"(qf[11]) :: "memory");
  unsigned cur = 0, nxt = 2 * SLOTB;
  for (int j = 0; j < nsteps; ++j) {
    asm volatile("s_waitcnt vmcnt(6) lgkmcnt(0)\n\ts_barrier" ::: "memory");
    dma3(rsK, voffK, ddst + nxt); dma3(rsV, voffV, ddst + nxt + TILEB);
    voffK += 32 * ROWB; voffV += 32 * ROWB;
    if (active && j < nt) {
      const unsigned ks = ring + cur;
      f32x16 s = f32x16{};
#pragma unroll
      for (int t = 0; t < 12; ++t) {
        const bf16x8 kf = *(const LDS(bf16x8)*)(ks + (kb ^ (32 * (t & 3))) + 4096 * (t >> 2));
        s = MFMA(kf, qf[t], s);
      }
      const int rem = nkeys - 32 * j;
      if (rem < 32) {
#pragma unroll
        for (int r = 0; r < 16; ++r) if ((r & 3) + 8 * (r >> 2) + 4 * hi >= rem) s[r] = -INFINITY;
      }
      float mx = MX3(s[0], s[1], s[2]), my = MX3(s[3], s[4], s[5]);
      mx = MX3(mx, s[6], s[7]); my = MX3(my, s[8], s[9]); mx = MX3(mx, s[10], s[11]); my = MX3(my, s[12], s[13]); mx = MX3(mx, s[14], s[15]);
      mx = swap_max(__builtin_fmaxf(mx, my)) * CEXP;
      if (__any(mx - mref > THR)) {
        const float mn = __builtin_fmaxf(mref, mx), al = __builtin_amdgcn_exp2f(mref - mn);
        mref = mn; lsum *= al;
#pragma unroll
        for (int d = 0; d < 6; ++d) o[d] *= al;
      }
      float ps = 0.f;
#pragma unroll
      for (int r = 0; r < 16; ++r) { s[r] = __builtin_amdgcn_exp2f(__builtin_fmaf(s[r], CEXP, -mref)); ps += s[r]; }
      lsum += ps;
      const u32x4 p0 = {cvtpk(s[0], s[1]), cvtpk(s[2], s[3]), cvtpk(s[4], s[5]), cvtpk(s[6], s[7])};
      const u32x4 p1 = {cvtpk(s[8], s[9]), cvtpk(s[10], s[11]), cvtpk(s[12], s[13]), cvtpk(s[14], s[15])};
      const bf16x8 pb0 = __builtin_bit_cast(bf16x8, p0), pb1 = __builtin_bit_cast(bf16x8, p1);
#pragma unroll
      for (int d = 0; d < 6; ++d) {
        const unsigned va = ks + (vb ^ (64 * (d & 1))) + 4096 * (d >> 1);
        const s16x4 a0 = vtr(va), a1 = vtr(va + 1024), a2 = vtr(va + 2048), a3 = vtr(va + 3072);
        o[d] = MFMA(__builtin_shufflevector(a0, a1, 0, 1, 2, 3, 4, 5, 6, 7), pb0, o[d]);
        o[d] = MFMA(__builtin_shufflevector(a2, a3, 0, 1, 2, 3, 4, 5, 6, 7), pb1, o[d]);
      }
    }
    nxt = cur; cur = cur == 2 * SLOTB ? 0 : cur + SLOTB;
  }
  asm volatile("s_waitcnt vmcnt(0)" ::: "memory");
  lsum = swap_add(lsum);
  const __amdgpu_buffer_rsrc_t rsO = mk_rsrc(O, qoff, qbytes);
  const unsigned sw = r32 * 128 + 8 * hi, sz = r32 & 7;
  const int srr = lane >> 3, sk = lane & 7;
  if (split) {
    asm volatile("s_barrier" ::: "memory");
    const unsigned fr0 = cur, fr1 = cur == 2 * SLOTB ? 0 : cur + SLOTB;
    const unsigned xw = ring + ((wq & 2) ? fr1 : fr0) + (wq & 1) * TILEB + lane * 16;
    const unsigned xr = lds0 + (half ^ 1) * HALFB + ((wq & 2) ? fr1 : fr0) + (wq & 1) * TILEB + lane * 16;
    *(LDS(f32x2)*)(lds0 + ML_OFF + wid * 512 + lane * 8) = f32x2{mref, lsum};
#pragma unroll
    for (int d = 0; d < 3; ++d)
#pragma unroll
      for (int t = 0; t < 4; ++t) {
        const f32x16& x = half ? o[d] : o[d + 3];
        *(LDS(f32x4)*)(xw + (4 * d + t) * 1024) = f32x4{x[4 * t], x[4 * t + 1], x[4 * t + 2], x[4 * t + 3]};
      }
    asm volatile("s_waitcnt lgkmcnt(0)\n\ts_barrier" ::: "memory");
    const f32x2 ml = *(const LDS(f32x2)*)(lds0 + ML_OFF + (wid ^ 4) * 512 + lane * 8);
    const float ms = __builtin_fmaxf(mref, ml[0]);
    float fa = __builtin_amdgcn_exp2f(mref - ms), fb = __builtin_amdgcn_exp2f(ml[0] - ms);
    const float inv = __builtin_amdgcn_rcpf(fa * lsum + fb * ml[1]);
    fa *= inv; fb *= inv;
    const unsigned stg = lds0 + (wq >> 1) * HALFB + nxt + (wq & 1) * TILEB;
#pragma unroll
    for (int d = 0; d < 3; ++d)
#pragma unroll
      for (int t = 0; t < 4; ++t) {
        const f32x16& x = half ? o[d + 3] : o[d];
        const f32x4 y = *(const LDS(f32x4)*)(xr + (4 * d + t) * 1024);
        const int db = d + 3 * half;
        const u32x2 w = {cvtpk(x[4 * t] * fa + y[0] * fb, x[4 * t + 1] * fa + y[1] * fb), cvtpk(x[4 * t + 2] * fa + y[2] * fb, x[4 * t + 3] * fa + y[3] * fb)};
        *(LDS(u32x2)*)(stg + (db >> 1) * 4096 + sw + 16 * ((4 * (db & 1) + t) ^ sz)) = w;
      }
    asm volatile("s_waitcnt lgkmcnt(0)\n\ts_barrier" ::: "memory");
#pragma unroll
    for (int a = 0; a < 2; ++a)
#pragma unroll
      for (int p = 0; p < 3; ++p) {
        const int row = 16 * half + 8 * a + srr;
        const u32x4 w = *(const LDS(u32x4)*)(stg + p * 4096 + row * 128 + 16 * (sk ^ srr));
        __builtin_amdgcn_raw_buffer_store_b128(w, rsO, (32 * wq + row) * ROWB + p * 128 + 16 * sk, 0, 0);
      }
  } else {
    asm volatile("s_barrier" ::: "memory");
    if (!active) return;
    const unsigned fr0 = cur, fr1 = cur == 2 * SLOTB ? 0 : cur + SLOTB;
    const unsigned stg = ring + ((wq & 2) ? fr1 : fr0) + (wq & 1) * TILEB;
    const float inv = __builtin_amdgcn_rcpf(lsum);
#pragma unroll
    for (int db = 0; db < 6; ++db)
#pragma unroll
      for (int t = 0; t < 4; ++t) {
        const u32x2 w = {cvtpk(o[db][4 * t] * inv, o[db][4 * t + 1] * inv), cvtpk(o[db][4 * t + 2] * inv, o[db][4 * t + 3] * inv)};
        *(LDS(u32x2)*)(stg + (db >> 1) * 4096 + sw + 16 * ((4 * (db & 1) + t) ^ sz)) = w;
      }
    asm volatile("s_waitcnt lgkmcnt(0)" ::: "memory");
#pragma unroll
    for (int a = 0; a < 4; ++a)
#pragma unroll
      for (int p = 0; p < 3; ++p) {
        const int row = 8 * a + srr;
        const u32x4 w = *(const LDS(u32x4)*)(stg + p * 4096 + row * 128 + 16 * (sk ^ srr));
        __builtin_amdgcn_raw_buffer_store_b128(w, rsO, (32 * wq + row) * ROWB + p * 128 + 16 * sk, 0, 0);
      }
  }
}

extern "C" void kernel_launch(void* const* d_in, const int* in_sizes, int n_in,
                              void* d_out, int out_size, void* d_ws, size_t ws_size,
                              hipStream_t stream) {
  const int T = in_sizes[0] / (NH * HD), nseq = in_sizes[3] - 1;
  int G = (T / 128 + nseq + 1) / 2;
  if (G < 16) G = 16;
  (void)hipFuncSetAttribute((const void*)varlen_attn_kernel, hipFuncAttributeMaxDynamicSharedMemorySize, (int)LDS_BYTES);
  hipLaunchKernelGGL(varlen_attn_kernel, dim3(NH * G), dim3(512), LDS_BYTES, stream,
                     (const __hip_bfloat16*)d_in[0], (const __hip_bfloat16*)d_in[1], (const __hip_bfloat16*)d_in[2],
                     (const int*)d_in[3], (__hip_bfloat16*)d_out, nseq, G);
}
```

```cpp
#include <hip/hip_runtime.h>
#include <hip/hip_bf16.h>

typedef __attribute__((ext_vector_type(8))) short bf16x8;
typedef __attribute__((ext_vector_type(4))) short s16x4;
typedef __attribute__((ext_vector_type(16))) float f32x16;
typedef __attribute__((ext_vector_type(4))) float f32x4;
typedef __attribute__((ext_vector_type(2))) float f32x2;
typedef __attribute__((ext_vector_type(4))) unsigned u32x4;
typedef __attribute__((ext_vector_type(2))) unsigned u32x2;
typedef __attribute__((ext_vector_type(2))) __bf16 bf16x2;
#define LDS(T) __attribute__((address_space(3))) T
#define DI __device__ __forceinline__

constexpr int NH = 16, HD = 192;
constexpr unsigned ROWB = NH * HD * 2;
constexpr unsigned HEADB = HD * 2;
constexpr unsigned TILEB = 32 * HEADB;
constexpr unsigned SLOTB = 2 * TILEB;
constexpr unsigned HALFB = 3 * SLOTB;
constexpr unsigned ML_OFF = 2 * HALFB;
constexpr unsigned LDS_BYTES = ML_OFF + 8 * 512;
constexpr float CEXP = 0.07216878364870322f * 1.4426950408889634f;
constexpr float THR = 8.0f;

DI unsigned rfl(unsigned x) { return __builtin_amdgcn_readfirstlane(x); }
DI unsigned cvtpk(float lo, float hi) { f32x2 v = {lo, hi}; bf16x2 b = __builtin_convertvector(v, bf16x2); return __builtin_bit_cast(unsigned, b); }
DI float swap_max(float x) { auto r = __builtin_amdgcn_permlane32_swap(__float_as_uint(x), __float_as_uint(x), false, false); return __builtin_fmaxf(__uint_as_float(r[0]), __uint_as_float(r[1])); }
DI float swap_add(float x) { auto r = __builtin_amdgcn_permlane32_swap(__float_as_uint(x), __float_as_uint(x), false, false); return __uint_as_float(r[0]) + __uint_as_float(r[1]); }
DI __amdgpu_buffer_rsrc_t mk_rsrc(const void* base, unsigned long long off, unsigned bytes) {
  unsigned long long a = (unsigned long long)base + off;
  unsigned lo = rfl((unsigned)a), hi = rfl((unsigned)(a >> 32));
  return __builtin_amdgcn_make_buffer_rsrc((void*)(((unsigned long long)hi << 32) | lo), (short)0, (int)rfl(bytes), 0x00020000);
}
DI void dma3(__amdgpu_buffer_rsrc_t rs, unsigned voff, unsigned ldsdst) {
  unsigned keep;
  asm volatile("s_mov_b32 %0, m0\n\t"
               "s_mov_b32 m0, %3\n\ts_nop 0\n\t"
               "buffer_load_dwordx4 %1, %2, 0 offen lds\n\t"
               "s_add_u32 m0, m0, 3968\n\ts_nop 0\n\t"
               "buffer_load_dwordx4 %1, %2, 0 offen offset:128 lds\n\t"
               "s_add_u32 m0, m0, 3968\n\ts_nop 0\n\t"
               "buffer_load_dwordx4 %1, %2, 0 offen offset:256 lds\n\t"
               "s_mov_b32 m0, %0"
               : "=&s"(keep) : "v"(voff), "s"(rs), "s"(ldsdst) : "memory", "scc");
}
#define STAMP(i)
#define PROF_ARG
#define PROF_PASS
#define MFMA(a, b, c) __builtin_amdgcn_mfma_f32_32x32x16_bf16((a), (b), (c), 0, 0, 0)
#define MX3(a, b, c) __builtin_fmaxf(__builtin_fmaxf((a), (b)), (c))
typedef short v4i16_t __attribute__((ext_vector_type(4)));
DI s16x4 vtr(unsigned a) { return __builtin_bit_cast(s16x4, __builtin_amdgcn_ds_read_tr16_b64_v4i16((LDS(v4i16_t)*)a)); }

__global__ __launch_bounds__(512, 2) void varlen_attn_kernel(const __hip_bfloat16* __restrict__ Q, const __hip_bfloat16* __restrict__ K,
                                                            const __hip_bfloat16* __restrict__ V, const int* __restrict__ cu,
                                                            __hip_bfloat16* __restrict__ O, int nseq, int G PROF_ARG) {
  extern __shared__ __attribute__((aligned(16))) char smem[];
  const unsigned lds0 = (unsigned)(size_t)smem;
  const int lane = threadIdx.x & 63;
  const int wid = rfl(threadIdx.x >> 6), half = wid >> 2, wq = wid & 3;
  const int b = blockIdx.x;
  STAMP(0);
  const int head = 2 * (b & 7) + ((b >> 3) & 1), g = b >> 4;

  int c0 = 0, len = 0;
  if (lane < nseq) { c0 = cu[lane]; len = cu[lane + 1] - c0; }
  const int nb = (len + 127) >> 7;
  int start = 0, J = 0;
  for (int j = 0; j < nseq; ++j) {
    const int lj = __builtin_amdgcn_readlane(len, j), nj = (lj + 127) >> 7;
    start += (lj > len || (lj == len && j < lane)) ? nj : 0;
    J += nj;
  }
  const int S = min(J, 2 * G - J);
  const bool split = g < S;
  const int u = split ? g : (half == 0 ? g : J - 1 - (g - S));
  if (g >= J || (!split && half == 1 && u <= g)) return;
  const unsigned long long hit = __ballot(lane < nseq && start <= u && u < start + nb);
  const int si = __builtin_ctzll(hit);
  const int seq0 = __builtin_amdgcn_readlane(c0, si), L = __builtin_amdgcn_readlane(len, si);
  const int qrow = (u - __builtin_amdgcn_readlane(start, si)) << 7;
  const int nq = min(128, L - qrow);
  const int ntile = (L + 31) >> 5;
  const int n0 = (ntile + 1) >> 1;
  const int t0 = (split && half) ? n0 : 0;
  const int nt = split ? (half ? ntile - n0 : n0) : ntile;
  const int nsteps = split ? n0 : ntile;
  const int nkeys = min(L - t0 * 32, nt * 32);

  const unsigned long long kvoff = (unsigned long long)(seq0 + t0 * 32) * ROWB + head * HEADB;
  const unsigned kvbytes = nkeys > 0 ? (unsigned)(nkeys - 1) * ROWB + HEADB : 0u;
  const __amdgpu_buffer_rsrc_t rsK = mk_rsrc(K, kvoff, kvbytes), rsV = mk_rsrc(V, kvoff, kvbytes);
  const unsigned long long qoff = (unsigned long long)(seq0 + qrow) * ROWB + head * HEADB;
  const unsigned qbytes = (unsigned)(nq - 1) * ROWB + HEADB;
  const __amdgpu_buffer_rsrc_t rsQ = mk_rsrc(Q, qoff, qbytes);

  const int r32 = lane & 31, hi = lane >> 5;
  const unsigned ring = lds0 + half * HALFB;
  const int drow = 8 * wq + (lane >> 3), dk = lane & 7;
  unsigned voffK = drow * ROWB + 16 * (dk ^ ((drow >> 1) & 7));
  unsigned voffV = drow * ROWB + 16 * (dk ^ (((drow >> 1) & 1) << 2));
  const unsigned ddst = ring + wq * 1024;

  STAMP(1);
  dma3(rsK, voffK, ddst);
  bf16x8 qf[12];
  {
    const unsigned vq = (32 * wq + r32) * ROWB + 16 * hi;
#define QLD(s) asm volatile("buffer_load_dwordx4 %0, %1, %2, 0 offen offset:%3" : "=v"(qf[s]) : "v"(vq), "s"(rsQ), "i"(32 * (s)) : "memory")
    QLD(0); QLD(1); QLD(2); QLD(3); QLD(4); QLD(5); QLD(6); QLD(7); QLD(8); QLD(9); QLD(10); QLD(11);
#undef QLD
  }
  dma3(rsV, voffV, ddst + TILEB);
  voffK += 32 * ROWB; voffV += 32 * ROWB;
  dma3(rsK, voffK, ddst + SLOTB); dma3(rsV, voffV, ddst + SLOTB + TILEB);
  voffK += 32 * ROWB; voffV += 32 * ROWB;

  const unsigned kb = r32 * 128 + 16 * (hi ^ ((r32 >> 1) & 7));
  const unsigned vb = TILEB + (4 * hi + ((lane >> 2) & 3)) * 128 + 64 * ((lane >> 3) & 1) + 32 * ((lane >> 4) & 1) + 8 * (lane & 3);

  f32x16 o[6];
#pragma unroll
  for (int d = 0; d < 6; ++d) o[d] = f32x16{};
  float mref = -1e30f, lsum = 0.f;
  const bool active = 32 * wq < nq;

  asm volatile("s_waitcnt vmcnt(6)" : "+v"(qf[0]), "+v"(qf[1]), "+v"(qf[2]), "+v"(qf[3]), "+v"(qf[4]), "+v"(qf[5]), "+v"(qf[6]), "+v"(qf[7]),
               "+v"(qf[8]), "+v"(qf[9]), "+v"(qf[10]), "+v"(qf[11]) :: "memory");
  STAMP(2);
  unsigned cur = 0, nxt = 2 * SLOTB;
  for (int j = 0; j < nsteps; ++j) {
    asm volatile("s_waitcnt vmcnt(6) lgkmcnt(0)\n\ts_barrier" ::: "memory");
    dma3(rsK, voffK, ddst + nxt); dma3(rsV, voffV, ddst + nxt + TILEB);
    voffK += 32 * ROWB; voffV += 32 * ROWB;
    if (active && j < nt) {
      const unsigned ks = ring + cur;
      f32x16 s = f32x16{};
#pragma unroll
      for (int t = 0; t < 12; ++t) {
        const bf16x8 kf = *(const LDS(bf16x8)*)(ks + (kb ^ (32 * (t & 3))) + 4096 * (t >> 2));
        s = MFMA(kf, qf[t], s);
      }
      const int rem = nkeys - 32 * j;
      if (rem < 32) {
#pragma unroll
        for (int r = 0; r < 16; ++r) if ((r & 3) + 8 * (r >> 2) + 4 * hi >= rem) s[r] = -INFINITY;
      }
      float mx = MX3(s[0], s[1], s[2]), my = MX3(s[3], s[4], s[5]);
      mx = MX3(mx, s[6], s[7]); my = MX3(my, s[8], s[9]); mx = MX3(mx, s[10], s[11]); my = MX3(my, s[12], s[13]); mx = MX3(mx, s[14], s[15]);
      mx = swap_max(__builtin_fmaxf(mx, my)) * CEXP;
      if (__any(mx - mref > THR)) {
        const float mn = __builtin_fmaxf(mref, mx), al = __builtin_amdgcn_exp2f(mref - mn);
        mref = mn; lsum *= al;
#pragma unroll
        for (int d = 0; d < 6; ++d) o[d] *= al;
      }
      float ps = 0.f;
#pragma unroll
      for (int r = 0; r < 16; ++r) { s[r] = __builtin_amdgcn_exp2f(__builtin_fmaf(s[r], CEXP, -mref)); ps += s[r]; }
      lsum += ps;
      const u32x4 p0 = {cvtpk(s[0], s[1]), cvtpk(s[2], s[3]), cvtpk(s[4], s[5]), cvtpk(s[6], s[7])};
      const u32x4 p1 = {cvtpk(s[8], s[9]), cvtpk(s[10], s[11]), cvtpk(s[12], s[13]), cvtpk(s[14], s[15])};
      const bf16x8 pb0 = __builtin_bit_cast(bf16x8, p0), pb1 = __builtin_bit_cast(bf16x8, p1);
#pragma unroll
      for (int d = 0; d < 6; ++d) {
        const unsigned va = ks + (vb ^ (64 * (d & 1))) + 4096 * (d >> 1);
        const s16x4 a0 = vtr(va), a1 = vtr(va + 1024), a2 = vtr(va + 2048), a3 = vtr(va + 3072);
        o[d] = MFMA(__builtin_shufflevector(a0, a1, 0, 1, 2, 3, 4, 5, 6, 7), pb0, o[d]);
        o[d] = MFMA(__builtin_shufflevector(a2, a3, 0, 1, 2, 3, 4, 5, 6, 7), pb1, o[d]);
      }
    }
    nxt = cur; cur = cur == 2 * SLOTB ? 0 : cur + SLOTB;
    if (j < 10) STAMP(3 + j);
  }
  STAMP(13);
  asm volatile("s_waitcnt vmcnt(0)" ::: "memory");
  lsum = swap_add(lsum);
  const __amdgpu_buffer_rsrc_t rsO = mk_rsrc(O, qoff, qbytes);
  const unsigned sw = r32 * 128 + 8 * hi, sz = r32 & 7;
  const int srr = lane >> 3, sk = lane & 7;
  if (split) {
    asm volatile("s_barrier" ::: "memory");
    const unsigned fr0 = cur, fr1 = cur == 2 * SLOTB ? 0 : cur + SLOTB;
    const unsigned xw = ring + ((wq & 2) ? fr1 : fr0) + (wq & 1) * TILEB + lane * 16;
    const unsigned xr = lds0 + (half ^ 1) * HALFB + ((wq & 2) ? fr1 : fr0) + (wq & 1) * TILEB + lane * 16;
    *(LDS(f32x2)*)(lds0 + ML_OFF + wid * 512 + lane * 8) = f32x2{mref, lsum};
#pragma unroll
    for (int d = 0; d < 3; ++d)
#pragma unroll
      for (int t = 0; t < 4; ++t) {
        const f32x16& x = half ? o[d] : o[d + 3];
        *(LDS(f32x4)*)(xw + (4 * d + t) * 1024) = f32x4{x[4 * t], x[4 * t + 1], x[4 * t + 2], x[4 * t + 3]};
      }
    asm volatile("s_waitcnt lgkmcnt(0)\n\ts_barrier" ::: "memory");
    const f32x2 ml = *(const LDS(f32x2)*)(lds0 + ML_OFF + (wid ^ 4) * 512 + lane * 8);
    const float ms = __builtin_fmaxf(mref, ml[0]);
    float fa = __builtin_amdgcn_exp2f(mref - ms), fb = __builtin_amdgcn_exp2f(ml[0] - ms);
    const float inv = __builtin_amdgcn_rcpf(fa * lsum + fb * ml[1]);
    fa *= inv; fb *= inv;
    const unsigned stg = lds0 + (wq >> 1) * HALFB + nxt + (wq & 1) * TILEB;
#pragma unroll
    for (int d = 0; d < 3; ++d)
#pragma unroll
      for (int t = 0; t < 4; ++t) {
        const f32x16& x = half ? o[d + 3] : o[d];
        const f32x4 y = *(const LDS(f32x4)*)(xr + (4 * d + t) * 1024);
        const int db = d + 3 * half;
        const u32x2 w = {cvtpk(x[4 * t] * fa + y[0] * fb, x[4 * t + 1] * fa + y[1] * fb), cvtpk(x[4 * t + 2] * fa + y[2] * fb, x[4 * t + 3] * fa + y[3] * fb)};
        *(LDS(u32x2)*)(stg + (db >> 1) * 4096 + sw + 16 * ((4 * (db & 1) + t) ^ sz)) = w;
      }
    asm volatile("s_waitcnt lgkmcnt(0)\n\ts_barrier" ::: "memory");
#pragma unroll
    for (int a = 0; a < 2; ++a)
#pragma unroll
      for (int p = 0; p < 3; ++p) {
        const int row = 16 * half + 8 * a + srr;
        const u32x4 w = *(const LDS(u32x4)*)(stg + p * 4096 + row * 128 + 16 * (sk ^ srr));
        __builtin_amdgcn_raw_buffer_store_b128(w, rsO, (32 * wq + row) * ROWB + p * 128 + 16 * sk, 0, 0);
      }
  } else {
    asm volatile("s_barrier" ::: "memory");
    if (!active) return;
    const unsigned fr0 = cur, fr1 = cur == 2 * SLOTB ? 0 : cur + SLOTB;
    const unsigned stg = ring + ((wq & 2) ? fr1 : fr0) + (wq & 1) * TILEB;
    const float inv = __builtin_amdgcn_rcpf(lsum);
#pragma unroll
    for (int db = 0; db < 6; ++db)
#pragma unroll
      for (int t = 0; t < 4; ++t) {
        const u32x2 w = {cvtpk(o[db][4 * t] * inv, o[db][4 * t + 1] * inv), cvtpk(o[db][4 * t + 2] * inv, o[db][4 * t + 3] * inv)};
        *(LDS(u32x2)*)(stg + (db >> 1) * 4096 + sw + 16 * ((4 * (db & 1) + t) ^ sz)) = w;
      }
    asm volatile("s_waitcnt lgkmcnt(0)" ::: "memory");
#pragma unroll
    for (int a = 0; a < 4; ++a)
#pragma unroll
      for (int p = 0; p < 3; ++p) {
        const int row = 8 * a + srr;
        const u32x4 w = *(const LDS(u32x4)*)(stg + p * 4096 + row * 128 + 16 * (sk ^ srr));
        __builtin_amdgcn_raw_buffer_store_b128(w, rsO, (32 * wq + row) * ROWB + p * 128 + 16 * sk, 0, 0);
      }
  }
  STAMP(14);
  STAMP(15);
}

extern "C" void kernel_launch(void* const* d_in, const int* in_sizes, int n_in,
                              void* d_out, int out_size, void* d_ws, size_t ws_size,
                              hipStream_t stream) {
  const int T = in_sizes[0] / (NH * HD), nseq = in_sizes[3] - 1;
  int G = (T / 128 + nseq + 1) / 2;
  if (G < 16) G = 16;
  (void)hipFuncSetAttribute((const void*)varlen_attn_kernel, hipFuncAttributeMaxDynamicSharedMemorySize, (int)LDS_BYTES);
  hipLaunchKernelGGL(varlen_attn_kernel, dim3(NH * G), dim3(512), LDS_BYTES, stream,
                     (const __hip_bfloat16*)d_in[0], (const __hip_bfloat16*)d_in[1], (const __hip_bfloat16*)d_in[2],
                     (const int*)d_in[3], (__hip_bfloat16*)d_out, nseq, G PROF_PASS);
}
```

```cpp
#include <hip/hip_runtime.h>
#include <hip/hip_bf16.h>
#include <type_traits>

typedef __attribute__((ext_vector_type(8))) short bf16x8;
typedef __attribute__((ext_vector_type(4))) short s16x4;
typedef __attribute__((ext_vector_type(16))) float f32x16;
typedef __attribute__((ext_vector_type(4))) float f32x4;
typedef __attribute__((ext_vector_type(2))) float f32x2;
typedef __attribute__((ext_vector_type(4))) unsigned u32x4;
typedef __attribute__((ext_vector_type(2))) unsigned u32x2;
typedef __attribute__((ext_vector_type(2))) __bf16 bf16x2;
typedef __attribute__((ext_vector_type(2))) int i32x2;
#define LDS(T) __attribute__((address_space(3))) T
#define DI __device__ __forceinline__
#define SBAR() __builtin_amdgcn_sched_barrier(0)

constexpr int NH = 16, HD = 192;
constexpr unsigned ROWB = NH * HD * 2;
constexpr unsigned HEADB = HD * 2;
constexpr unsigned TILEB = 32 * HEADB;
constexpr unsigned SLOTB = 2 * TILEB;
constexpr unsigned HALFB = 3 * SLOTB;
constexpr unsigned ML_OFF = 2 * HALFB;
constexpr unsigned DUMP_OFF = ML_OFF + 8 * 512;
constexpr unsigned LDS_BYTES = DUMP_OFF + 9216;
constexpr unsigned TSTEP = 32 * ROWB;
constexpr float CEXP = 0.07216878364870322f * 1.4426950408889634f;
constexpr float THR = 8.0f;
#define CODE_PF 8192
#define KVPOL ""
#define QPOL "nt"

DI unsigned rfl(unsigned x) { return __builtin_amdgcn_readfirstlane(x); }
DI unsigned cvtpk(float lo, float hi) { f32x2 v = {lo, hi}; bf16x2 b = __builtin_convertvector(v, bf16x2); return __builtin_bit_cast(unsigned, b); }
DI float max3f(float a, float b, float c) { float r; asm("v_max3_f32 %0, %1, %2, %3" : "=v"(r) : "v"(a), "v"(b), "v"(c)); return r; }
DI float max2f(float a, float b) { float r; asm("v_max_f32_e32 %0, %1, %2" : "=v"(r) : "v"(a), "v"(b)); return r; }
DI float swap_add(float x) { auto r = __builtin_amdgcn_permlane32_swap(__float_as_uint(x), __float_as_uint(x), false, false); return __uint_as_float(r[0]) + __uint_as_float(r[1]); }
DI __amdgpu_buffer_rsrc_t mk_rsrc(const void* base, unsigned bytes) {
  return __builtin_amdgcn_make_buffer_rsrc((void*)base, (short)0, (int)bytes, 0x00020000);
}
#define DMA3(POL, rs, voff, soff, ldsdst) \
  asm volatile("s_mov_b32 m0, %3\n\ts_nop 0\n\t" \
               "buffer_load_dwordx4 %0, %1, %2 offen " POL " lds\n\t" \
               "s_add_u32 m0, m0, 3968\n\ts_nop 0\n\t" \
               "buffer_load_dwordx4 %0, %1, %2 offen offset:128 " POL " lds\n\t" \
               "s_add_u32 m0, m0, 3968\n\ts_nop 0\n\t" \
               "buffer_load_dwordx4 %0, %1, %2 offen offset:256 " POL " lds" \
               :: "v"(voff), "s"(rs), "s"(soff), "s"(ldsdst) : "memory", "scc")
template <int P> DI void dma1(__amdgpu_buffer_rsrc_t rs, unsigned voff, unsigned soff, unsigned ldsdst) {
  asm volatile("s_add_u32 m0, %3, %4\n\ts_nop 0\n\t"
               "buffer_load_dwordx4 %0, %1, %2 offen offset:%5 " KVPOL " lds"
               :: "v"(voff), "s"(rs), "s"(soff), "s"(ldsdst), "i"(3968 * P), "i"(128 * P) : "memory", "scc");
}
#define WAIT_BAR(N) asm volatile("s_waitcnt vmcnt(" #N ") lgkmcnt(0)\n\ts_barrier" ::: "memory")
DI int lane_id() { int l; asm volatile("v_mbcnt_lo_u32_b32 %0, -1, 0\n\tv_mbcnt_hi_u32_b32 %0, -1, %0" : "=v"(l)); return l; }
#define STAMP(i)
#define PROF_ARG
#define PROF_PASS
#define PROF_END()
#define MFMA(a, b, c) __builtin_amdgcn_mfma_f32_32x32x16_bf16((a), (b), (c), 0, 0, 0)
typedef short v4i16_t __attribute__((ext_vector_type(4)));
DI s16x4 vtr(unsigned a) { return __builtin_bit_cast(s16x4, __builtin_amdgcn_ds_read_tr16_b64_v4i16((LDS(v4i16_t)*)a)); }
DI bf16x8 ldf(unsigned a) { return *(const LDS(bf16x8)*)a; }
template <int CTRL> DI int dpp_add(int x) { return x + __builtin_amdgcn_update_dpp(0, x, CTRL, 0xf, 0xf, true); }

__global__ __launch_bounds__(512, 2) void varlen_attn_kernel(const __hip_bfloat16* __restrict__ Q, const __hip_bfloat16* __restrict__ K,
                                                            const __hip_bfloat16* __restrict__ V, const int* __restrict__ cu,
                                                            __hip_bfloat16* __restrict__ O, int nseq, int G PROF_ARG) {
  extern __shared__ __attribute__((aligned(1024))) char smem[];
  const unsigned lds0 = (unsigned)(size_t)(LDS(char)*)smem;
  const int lane = threadIdx.x & 63;
  const int wid = rfl(threadIdx.x >> 6), half = wid >> 2, wq = wid & 3;
  const int b = blockIdx.x;
  STAMP(0);
  asm volatile("" :: "s"(Q), "s"(K), "s"(V), "s"(cu), "s"(O), "s"(nseq), "s"(G));
  STAMP(4);
  const int head = 2 * (b & 7) + ((b >> 3) & 1), g = b >> 4;

  const int sa = min(lane >> 3, nseq), sc = min(lane & 7, nseq);
  const int a0 = cu[sa], a1 = cu[min(sa + 1, nseq)], c0 = cu[sc], c1 = cu[min(sc + 1, nseq)];
  if (wid < CODE_PF / 8192) {
    unsigned long long pc;
    asm volatile("s_getpc_b64 %0" : "=s"(pc));
    asm volatile("s_mov_b32 m0, %2\n\ts_nop 0\n\tglobal_load_lds_dword %0, %1"
                 :: "v"(threadIdx.x * 128), "s"(pc & ~127ull), "s"(lds0 + DUMP_OFF) : "memory");
  }
  unsigned voff, vq;
  { const int rr = lane >> 3, dk = lane & 7, drow = 8 * wq + rr;
    voff = drow * ROWB + 16 * (dk ^ (half ? (((drow >> 1) & 1) << 2) : ((drow >> 1) & 7)));
    vq = rr * ROWB + 16 * (dk ^ (rr >> 1));
    asm volatile("" : "+v"(voff), "+v"(vq)); }
  const int la = a1 - a0, lc = c1 - c0;
  STAMP(5);
  const int nb2a = (la + 255) >> 8, nbiga = ((la + 127) >> 7) - nb2a;
  const int nb2c = (lc + 255) >> 8, nbigc = ((lc + 127) >> 7) - nb2c;
  int bef = (8 * lc - (lane & 7) > 8 * la - (lane >> 3)) ? nb2c + (nbigc << 16) : 0;
  int tot = nb2c;
  bef = dpp_add<0xB1>(bef); tot = dpp_add<0xB1>(tot);
  bef = dpp_add<0x4E>(bef); tot = dpp_add<0x4E>(tot);
  bef = dpp_add<0x141>(bef); tot = dpp_add<0x141>(tot);
  const int spare = G - tot, bigbef = bef >> 16;
  const int nsplit = min(max(spare - bigbef, 0), nbiga);
  const int start = (bef & 0xffff) + min(spare, bigbef);
  const unsigned long long hit = __ballot(start <= g && g < start + nb2a + nsplit);
  if (hit == 0) return;
  const int si = __builtin_ctzll(hit);
  const int seq0 = __builtin_amdgcn_readlane(a0, si), L = __builtin_amdgcn_readlane(la, si);
  const int x = g - __builtin_amdgcn_readlane(start, si), ns = __builtin_amdgcn_readlane(nsplit, si);
  const int qrow = x < 2 * ns ? x << 7 : (x - ns) << 8;
  const int nq = min(x < 2 * ns ? 128 : 256, L - qrow);
  const bool split = nq <= 128;
  const int ntile = (L + 31) >> 5;
  const int nA = split ? (ntile + 1) >> 1 : ntile, nB = ntile - nA;
  const int hs = split ? half : 0;
  const int nt = hs ? nB : nA;
  const int nkeys = hs ? L - 32 * nA : min(L, 32 * nA);
  const int qt = split ? wq : wid;

  const char* kvbase = (const char*)(half ? V : K) + head * HEADB;
  const __amdgpu_buffer_rsrc_t rsA = mk_rsrc(kvbase, (unsigned)(seq0 + min(L, 32 * nA) - 1) * ROWB + HEADB);
  const __amdgpu_buffer_rsrc_t rsB = mk_rsrc(kvbase, (unsigned)(seq0 + L - 1) * ROWB + HEADB);
  unsigned sA = (unsigned)seq0 * ROWB;
  const unsigned sAB = (unsigned)nA * TSTEP;
  const unsigned sQ = (unsigned)(seq0 + qrow) * ROWB, qbytes = sQ + (unsigned)(nq - 1) * ROWB + HEADB;
  const __amdgpu_buffer_rsrc_t rsQ = mk_rsrc((const char*)Q + head * HEADB, qbytes);

  const unsigned ddst = lds0 + half * TILEB + wq * 1024, dump = lds0 + DUMP_OFF;
  const unsigned qlds = lds0 + (split ? (wq >> 1) * HALFB + 2 * SLOTB + (wq & 1) * TILEB : 2 * SLOTB + wid * TILEB);
#define TILE_DMA(t, slot) do { DMA3(KVPOL, rsA, voff, sA, (t) < nA ? ddst + (slot) : dump); DMA3(KVPOL, rsB, voff, sA + sAB, (t) < nB ? ddst + (slot) + HALFB : dump); sA += TSTEP; } while (0)
  STAMP(1);
  if (half == 0) TILE_DMA(0, 0);
  {
    const int c = split ? 2 * half : 0;
    const unsigned sq = sQ + (32 * qt + 8 * c) * ROWB;
    DMA3(QPOL, rsQ, vq, sq, qlds + c * 1024);
    DMA3(QPOL, rsQ, vq ^ 64, sq + 8 * ROWB, qlds + c * 1024 + 1024);
    if (!split) { DMA3(QPOL, rsQ, vq, sq + 16 * ROWB, qlds + 2048); DMA3(QPOL, rsQ, vq ^ 64, sq + 24 * ROWB, qlds + 3072); }
  }
  if (half) TILE_DMA(0, 0);
  TILE_DMA(1, SLOTB);

  const unsigned ring = lds0 + hs * HALFB;
  f32x16 o[6];
#pragma unroll
  for (int d = 0; d < 6; ++d) { o[d] = f32x16{}; asm volatile("" : "+v"(o[d])); }
  float mref = -1e30f, lsum = 0.f;
  const bool active = 32 * qt < nq;
  const int ntw = active ? nt : 0;
  unsigned dsl = half ? 2 * SLOTB : 0;
  int td = 3 - half;
#define NEXT_SLOT(x) ((x) == 2 * SLOTB ? 0 : (x) + SLOTB)
  if (half) WAIT_BAR(12); else WAIT_BAR(6);
  STAMP(2);
  if (ntw > 0) {
    const int lane = lane_id(), r32 = lane & 31, hi = lane >> 5;
    const unsigned kb = r32 * 128 + 16 * (hi ^ ((r32 >> 1) & 7));
    const unsigned kbr = ring + kb;
    const unsigned vbase = ring + TILEB + (4 * hi + ((lane >> 2) & 3)) * 128 + 64 * (((lane >> 3) & 1) ^ half) + 32 * ((lane >> 4) & 1) + 8 * (lane & 3);
    const int rem = nkeys - 4 * hi;
    bf16x8 qf[12], kw[4];
    s16x4 va[8];
    f32x16 s, x;
    u32x4 p0, p1;
    unsigned kn, kn1, kn2, kn3, v0, v1;
    unsigned cur = 0, nx1 = SLOTB;
#define ORD(i) ((i) % 6 < 3 ? 2 * ((i) % 6) + 1 : 2 * ((i) % 6 - 3))
#define VRD(i) do { const unsigned a_ = ((ORD(i) & 1) ? v1 : v0) + 4096 * (ORD(i) >> 1) + 2048 * ((i) / 6); \
      va[2 * ((i) & 3)] = vtr(a_); va[2 * ((i) & 3) + 1] = vtr(a_ + 1024); } while (0)
#define VFR(i) __builtin_shufflevector(va[2 * ((i) & 3)], va[2 * ((i) & 3) + 1], 0, 1, 2, 3, 4, 5, 6, 7)
#define KRD(t) kw[(t) & 3] = ldf((((t) & 3) == 0 ? kn : ((t) & 3) == 1 ? kn1 : ((t) & 3) == 2 ? kn2 : kn3) + 4096 * ((t) >> 2))
#define KADDR(slot) do { kn = kbr + (slot); kn1 = kn ^ 32; kn2 = kn ^ 64; kn3 = kn ^ 96; } while (0)
#define VADDR(slot) do { v0 = vbase + (slot); v1 = (vbase ^ 64) + (slot); } while (0)
#define QK(t) s = MFMA(kw[(t) & 3], qf[t], (t) == 0 ? f32x16{} : s)
#define PV(i) o[ORD(i)] = MFMA(VFR(i), __builtin_bit_cast(bf16x8, (i) < 6 ? p0 : p1), o[ORD(i)])
#define EX2(r) do { x[r] = __builtin_amdgcn_exp2f(x[r]); x[(r) + 1] = __builtin_amdgcn_exp2f(x[(r) + 1]); } while (0)
#define AD2(r) do { lsum += x[r]; lsum += x[(r) + 1]; } while (0)
#define CV2(P, i, r) do { P[i] = cvtpk(x[r], x[(r) + 1]); P[(i) + 1] = cvtpk(x[(r) + 2], x[(r) + 3]); asm volatile("" : "+v"(P)); } while (0)
#define FM4(r) do { x[r] = __builtin_fmaf(s[r], CEXP, -mref); x[(r) + 1] = __builtin_fmaf(s[(r) + 1], CEXP, -mref); \
      x[(r) + 2] = __builtin_fmaf(s[(r) + 2], CEXP, -mref); x[(r) + 3] = __builtin_fmaf(s[(r) + 3], CEXP, -mref); } while (0)
#define PIN() do { asm volatile("" : "+v"(x), "+v"(lsum)); SBAR(); } while (0)
#define MX3(a, b, c) __builtin_fmaxf(__builtin_fmaxf((a), (b)), (c))
#define MASK(t) do { if (__builtin_expect(nkeys - 32 * (t) < 32, 0)) { \
      _Pragma("unroll") for (int r = 0; r < 16; ++r) s[r] = ((r & 3) + 8 * (r >> 2) + 32 * (t) < rem) ? s[r] : -INFINITY; } } while (0)
#define ROWMAX(m) do { auto r_ = __builtin_amdgcn_permlane32_swap(__float_as_uint(m), __float_as_uint(m), false, false); \
      m = max2f(__uint_as_float(r_[0]), __uint_as_float(r_[1])) * CEXP; } while (0)
    {
      bf16x8 k0[12];
#pragma unroll
      for (int t = 0; t < 12; ++t) { qf[t] = ldf(((qlds + kb) ^ (32 * (t & 3))) + 4096 * (t >> 2)); k0[t] = ldf((kbr ^ (32 * (t & 3))) + 4096 * (t >> 2)); }
      SBAR();
#pragma unroll
      for (int t = 0; t < 12; ++t) s = MFMA(k0[t], qf[t], t == 0 ? f32x16{} : s);
      KADDR(nx1);
      SBAR();
      if (half) WAIT_BAR(6); else WAIT_BAR(0);
      KRD(0); KRD(1); KRD(2); KRD(3);
      if (half == 0) TILE_DMA(2, 2 * SLOTB);
      MASK(0);
      float mx = MX3(MX3(MX3(s[0], s[1], s[2]), MX3(s[3], s[4], s[5]), MX3(s[6], s[7], s[8])), MX3(s[9], s[10], s[11]), MX3(MX3(s[12], s[13], s[14]), s[15], s[15]));
      ROWMAX(mx); mref = mx;
      FM4(0); FM4(4); FM4(8); FM4(12); PIN();
    }
    STAMP(3);
    for (int j = 1; j < ntw; ++j) {
      const unsigned dd = ddst + dsl;
      const unsigned dA = td < nA ? dd : dump, dB = td < nB ? dd + HALFB : dump, sB = sA + sAB;
      SBAR();
      QK(0);  KRD(4);  EX2(0);           dma1<0>(rsA, voff, sA, dA); PIN();
      QK(1);  KRD(5);  EX2(2);  AD2(0);  PIN();
      QK(2);  KRD(6);  EX2(4);  AD2(2);  dma1<1>(rsA, voff, sA, dA); PIN();
      QK(3);  KRD(7);  EX2(6);  AD2(4);  PIN();
      QK(4);  KRD(8);  EX2(8);  AD2(6);  dma1<2>(rsA, voff, sA, dA); PIN();
      QK(5);  KRD(9);  EX2(10); AD2(8);  PIN();
      QK(6);  KRD(10); EX2(12); AD2(10); dma1<0>(rsB, voff, sB, dB); PIN();
      QK(7);  KRD(11); EX2(14); AD2(12); VADDR(cur); PIN();
      QK(8);  VRD(0);  CV2(p0, 0, 0);  AD2(14); PIN();
      QK(9);  VRD(1);  CV2(p0, 2, 4);  dma1<1>(rsB, voff, sB, dB); SBAR();
      QK(10); VRD(2);  CV2(p1, 0, 8);  SBAR();
      QK(11); VRD(3);  CV2(p1, 2, 12); dma1<2>(rsB, voff, sB, dB); SBAR();
      PV(0); VRD(4); SBAR();
      PV(1); VRD(5); asm volatile("" ::: "memory"); SBAR();
      MASK(j);
      float m0_ = MX3(s[0], s[1], s[2]), m1_ = MX3(s[3], s[4], s[5]), m2_ = MX3(s[6], s[7], s[8]), m3_ = MX3(s[9], s[10], s[11]);
      asm volatile("" : "+v"(m0_), "+v"(m1_), "+v"(m2_), "+v"(m3_)); SBAR();
      PV(2); VRD(6);
      float mx = MX3(MX3(m0_, m1_, m2_), m3_, MX3(MX3(s[12], s[13], s[14]), s[15], s[15]));
      asm volatile("" : "+v"(mx)); SBAR();
      PV(3); VRD(7); asm volatile("" ::: "memory"); SBAR();
      float al = 1.f; bool pend = false;
      if (__builtin_expect(__any(__builtin_fmaf(mx, CEXP, -mref) > THR), 0)) {
        ROWMAX(mx);
        const float mn = max2f(mref, mx);
        al = __builtin_amdgcn_exp2f(mref - mn); mref = mn; lsum *= al; pend = true;
      }
      PV(4); VRD(8);  FM4(0);  PIN();
      PV(5); VRD(9);  FM4(4);  PIN();
      PV(6); VRD(10); FM4(8);  PIN();
      PV(7); VRD(11); FM4(12); PIN();
      cur = nx1; nx1 = NEXT_SLOT(nx1); dsl = NEXT_SLOT(dsl); ++td; sA += TSTEP;
      PV(8); KADDR(nx1); asm volatile("" : "+v"(kn), "+v"(kn1), "+v"(kn2), "+v"(kn3)); SBAR();
      PV(9); SBAR();
      WAIT_BAR(6); SBAR();
      PV(10); SBAR(); KRD(0); KRD(1); KRD(2); KRD(3); asm volatile("" ::: "memory"); SBAR();
      PV(11); SBAR();
      if (__builtin_expect(pend, 0)) {
#pragma unroll
        for (int d = 0; d < 6; ++d) o[d] *= al;
      }
    }
    VADDR(cur);
    VRD(0); VRD(1); VRD(2); VRD(3);
    EX2(0); EX2(2); EX2(4); EX2(6); CV2(p0, 0, 0); CV2(p0, 2, 4); PIN();
    PV(0); VRD(4); EX2(8);  PIN();
    PV(1); VRD(5); EX2(10); PIN();
    PV(2); VRD(6); EX2(12); PIN();
    PV(3); VRD(7); EX2(14); PIN();
    PV(4); VRD(8); CV2(p1, 0, 8); SBAR();
    PV(5); VRD(9); CV2(p1, 2, 12); SBAR();
    PV(6); VRD(10); AD2(0); AD2(2); PIN();
    PV(7); VRD(11); AD2(4); AD2(6); PIN();
    PV(8); AD2(8); AD2(10); PIN();
    PV(9); AD2(12); AD2(14); PIN();
    PV(10); PV(11); SBAR();
    if (ntw < nA) WAIT_BAR(0);
  } else {
    if (half) WAIT_BAR(6); else { WAIT_BAR(0); TILE_DMA(2, 2 * SLOTB); }
    for (int j = 1; j < nA; ++j) {
      TILE_DMA(td, dsl); dsl = NEXT_SLOT(dsl); ++td;
      WAIT_BAR(6);
    }
  }
  STAMP(13);
  lsum = swap_add(lsum);
  const __amdgpu_buffer_rsrc_t rsO = mk_rsrc((char*)O + head * HEADB, qbytes);
  {
  const int lane = lane_id(), r32 = lane & 31, hi = lane >> 5, rr = lane >> 3, dk = lane & 7;
  if (split) {
    const unsigned xo = (wq == 3 ? (unsigned)(nA % 3) * SLOTB + TILEB : wq * SLOTB) + lane * 16;
    const unsigned xw = lds0 + half * HALFB + xo, xr = lds0 + (half ^ 1) * HALFB + xo;
    *(LDS(f32x2)*)(lds0 + ML_OFF + wid * 512 + lane * 8) = f32x2{mref, lsum};
#pragma unroll
    for (int p = 0; p < 3; ++p)
#pragma unroll
      for (int t = 0; t < 4; ++t) {
        const f32x16& y = o[2 * p + 1];
        *(LDS(f32x4)*)(xw + (4 * p + t) * 1024) = f32x4{y[4 * t], y[4 * t + 1], y[4 * t + 2], y[4 * t + 3]};
      }
    asm volatile("s_waitcnt lgkmcnt(0)\n\ts_barrier" ::: "memory");
    if (active) {
      const f32x2 ml = *(const LDS(f32x2)*)(lds0 + ML_OFF + (wid ^ 4) * 512 + lane * 8);
      f32x4 y[12];
#pragma unroll
      for (int i = 0; i < 12; ++i) y[i] = *(const LDS(f32x4)*)(xr + i * 1024);
      SBAR();
      const float ms = max2f(mref, ml[0]);
      float fa = __builtin_amdgcn_exp2f(mref - ms), fb = __builtin_amdgcn_exp2f(ml[0] - ms);
      const float inv = __builtin_amdgcn_rcpf(fa * lsum + fb * ml[1]);
      fa *= inv; fb *= inv;
      u32x2 w[12];
#pragma unroll
      for (int i = 0; i < 12; ++i) {
        const f32x16& z = o[2 * (i >> 2)]; const int t = i & 3;
        w[i] = u32x2{cvtpk(z[4 * t] * fa + y[i][0] * fb, z[4 * t + 1] * fa + y[i][1] * fb), cvtpk(z[4 * t + 2] * fa + y[i][2] * fb, z[4 * t + 3] * fa + y[i][3] * fb)};
      }
      SBAR();
      const unsigned stg = xr - lane * 16;
      const unsigned sw = stg + r32 * 64 + 8 * hi + 16 * ((r32 >> 1) & 3);
#pragma unroll
      for (int i = 0; i < 12; ++i) *(LDS(u32x2)*)((sw ^ (16 * (i & 3))) + (i >> 2) * 2048) = w[i];
      asm volatile("s_waitcnt lgkmcnt(0)" ::: "memory");
      const int sr = lane >> 2, sk = lane & 3;
      const unsigned srd = stg + sr * 64 + 16 * (sk ^ ((sr >> 1) & 3));
      u32x4 z[6];
#pragma unroll
      for (int i = 0; i < 6; ++i) z[i] = *(const LDS(u32x4)*)(srd + i * 1024);
      SBAR();
      const unsigned go = (32 * wq + sr) * ROWB + 64 * half + 16 * sk;
#pragma unroll
      for (int i = 0; i < 6; ++i) __builtin_amdgcn_raw_buffer_store_b128(z[i], rsO, go + (i >> 1) * 128, sQ + (i & 1) * 16 * ROWB, 0);
    }
  } else if (active) {
    const unsigned stg = lds0 + (wid < 6 ? HALFB + wid * TILEB : (wid - 6) * SLOTB);
    const float inv = __builtin_amdgcn_rcpf(lsum);
    u32x2 w[24];
#pragma unroll
    for (int i = 0; i < 24; ++i) { const f32x16& z = o[i >> 2]; const int t = i & 3;
      w[i] = u32x2{cvtpk(z[4 * t] * inv, z[4 * t + 1] * inv), cvtpk(z[4 * t + 2] * inv, z[4 * t + 3] * inv)}; }
    SBAR();
    const unsigned sw = stg + r32 * 128 + 8 * hi + 16 * ((r32 & 7) ^ (4 * half));
#pragma unroll
    for (int i = 0; i < 24; ++i) *(LDS(u32x2)*)((sw ^ (16 * (i & 7))) + (i >> 3) * 4096) = w[i];
    asm volatile("s_waitcnt lgkmcnt(0)" ::: "memory");
    const unsigned srd = stg + rr * 128 + 16 * (dk ^ rr);
    u32x4 z[12];
#pragma unroll
    for (int i = 0; i < 12; ++i) z[i] = *(const LDS(u32x4)*)(srd + i * 1024);
    SBAR();
    const unsigned go = (32 * wid + rr) * ROWB + 16 * dk;
#pragma unroll
    for (int i = 0; i < 12; ++i) __builtin_amdgcn_raw_buffer_store_b128(z[i], rsO, go + (i >> 2) * 128, sQ + (i & 3) * 8 * ROWB, 0);
  }
  }
  STAMP(14);
  STAMP(15);
  PROF_END();
}

extern "C" void kernel_launch(void* const* d_in, const int* in_sizes, int n_in,
                              void* d_out, int out_size, void* d_ws, size_t ws_size,
                              hipStream_t stream) {
  const int T = in_sizes[0] / (NH * HD), nseq = in_sizes[3] - 1;
  (void)hipFuncSetAttribute((const void*)varlen_attn_kernel, hipFuncAttributeMaxDynamicSharedMemorySize, (int)LDS_BYTES);
  for (int s0 = 0; s0 < nseq; s0 += 8) {
    const int ns = nseq - s0 < 8 ? nseq - s0 : 8;
    int G = T / 256 + ns;
    if (G < 16) G = 16;
    hipLaunchKernelGGL(varlen_attn_kernel, dim3(NH * G), dim3(512), LDS_BYTES, stream,
                       (const __hip_bfloat16*)d_in[0], (const __hip_bfloat16*)d_in[1], (const __hip_bfloat16*)d_in[2],
                       (const int*)d_in[3] + s0, (__hip_bfloat16*)d_out, ns, G PROF_PASS);
  }
}
```

```cpp
#define SIDE 0
#define SIDE_COND (blockIdx.x == 0 && wid == 0)
#include <hip/hip_runtime.h>
#include <hip/hip_bf16.h>
#include <type_traits>

typedef __attribute__((ext_vector_type(8))) short bf16x8;
typedef __attribute__((ext_vector_type(4))) short s16x4;
typedef __attribute__((ext_vector_type(16))) float f32x16;
typedef __attribute__((ext_vector_type(4))) float f32x4;
typedef __attribute__((ext_vector_type(2))) float f32x2;
typedef __attribute__((ext_vector_type(4))) unsigned u32x4;
typedef __attribute__((ext_vector_type(2))) unsigned u32x2;
typedef __attribute__((ext_vector_type(2))) __bf16 bf16x2;
typedef __attribute__((ext_vector_type(2))) int i32x2;
#define LDS(T) __attribute__((address_space(3))) T
#define DI __device__ __forceinline__
#define SBAR() __builtin_amdgcn_sched_barrier(0)

constexpr int NH = 16, HD = 192;
constexpr unsigned ROWB = NH * HD * 2;
constexpr unsigned HEADB = HD * 2;
constexpr unsigned TILEB = 32 * HEADB;
constexpr unsigned SLOTB = 2 * TILEB;
constexpr unsigned HALFB = 3 * SLOTB;
constexpr unsigned ML_OFF = 2 * HALFB;
constexpr unsigned DUMP_OFF = ML_OFF + 8 * 512;
constexpr unsigned LDS_BYTES = DUMP_OFF + 9216;
constexpr unsigned TSTEP = 32 * ROWB;
constexpr float CEXP = 0.07216878364870322f * 1.4426950408889634f;
constexpr float THR = 8.0f;
#define CODE_PF 8192
#define KVPOL ""
#define QPOL ""

DI unsigned rfl(unsigned x) { return __builtin_amdgcn_readfirstlane(x); }
DI unsigned cvtpk(float lo, float hi) { f32x2 v = {lo, hi}; bf16x2 b = __builtin_convertvector(v, bf16x2); return __builtin_bit_cast(unsigned, b); }
DI float max3f(float a, float b, float c) { float r; asm("v_max3_f32 %0, %1, %2, %3" : "=v"(r) : "v"(a), "v"(b), "v"(c)); return r; }
DI float max2f(float a, float b) { float r; asm("v_max_f32_e32 %0, %1, %2" : "=v"(r) : "v"(a), "v"(b)); return r; }
DI float swap_add(float x) { auto r = __builtin_amdgcn_permlane32_swap(__float_as_uint(x), __float_as_uint(x), false, false); return __uint_as_float(r[0]) + __uint_as_float(r[1]); }
DI __amdgpu_buffer_rsrc_t mk_rsrc(const void* base, unsigned bytes) {
  return __builtin_amdgcn_make_buffer_rsrc((void*)base, (short)0, (int)bytes, 0x00020000);
}
#define DMA3(POL, rs, voff, soff, ldsdst) \
  asm volatile("s_mov_b32 m0, %3\n\ts_nop 0\n\t" \
               "buffer_load_dwordx4 %0, %1, %2 offen " POL " lds\n\t" \
               "s_add_u32 m0, m0, 3968\n\ts_nop 0\n\t" \
               "buffer_load_dwordx4 %0, %1, %2 offen offset:128 " POL " lds\n\t" \
               "s_add_u32 m0, m0, 3968\n\ts_nop 0\n\t" \
               "buffer_load_dwordx4 %0, %1, %2 offen offset:256 " POL " lds" \
               :: "v"(voff), "s"(rs), "s"(soff), "s"(ldsdst) : "memory", "scc")
template <int P> DI void dma1(__amdgpu_buffer_rsrc_t rs, unsigned voff, unsigned soff, unsigned ldsdst) {
  asm volatile("s_add_u32 m0, %3, %4\n\ts_nop 0\n\t"
               "buffer_load_dwordx4 %0, %1, %2 offen offset:%5 " KVPOL " lds"
               :: "v"(voff), "s"(rs), "s"(soff), "s"(ldsdst), "i"(3968 * P), "i"(128 * P) : "memory", "scc");
}
#define WAIT_BAR(N) asm volatile("s_waitcnt vmcnt(" #N ") lgkmcnt(0)\n\ts_barrier" ::: "memory")
DI int lane_id() { int l; asm volatile("v_mbcnt_lo_u32_b32 %0, -1, 0\n\tv_mbcnt_hi_u32_b32 %0, -1, %0" : "=v"(l)); return l; }
#define STAMP(i) do { if ((i) == 0 || (i) == SIDE) { unsigned long long t_; asm volatile("s_memrealtime %0\n\ts_waitcnt lgkmcnt(0)" : "=s"(t_) :: "memory"); if ((i) == 0) side0 = t_; side1 = t_; } } while (0)
#define PROF_ARG
#define PROF_PASS
#define PROF_END() do { if (SIDE_COND) { const unsigned long long tgt = side0 + 4000 + 10 * (side1 - side0); while (__builtin_amdgcn_s_memrealtime() < tgt) __builtin_amdgcn_s_sleep(4); } } while (0)
#define MFMA(a, b, c) __builtin_amdgcn_mfma_f32_32x32x16_bf16((a), (b), (c), 0, 0, 0)
typedef short v4i16_t __attribute__((ext_vector_type(4)));
DI s16x4 vtr(unsigned a) { return __builtin_bit_cast(s16x4, __builtin_amdgcn_ds_read_tr16_b64_v4i16((LDS(v4i16_t)*)a)); }
DI bf16x8 ldf(unsigned a) { return *(const LDS(bf16x8)*)a; }
template <int CTRL> DI int dpp_add(int x) { return x + __builtin_amdgcn_update_dpp(0, x, CTRL, 0xf, 0xf, true); }

__global__ __launch_bounds__(512, 2) void varlen_attn_kernel(const __hip_bfloat16* __restrict__ Q, const __hip_bfloat16* __restrict__ K,
                                                            const __hip_bfloat16* __restrict__ V, const int* __restrict__ cu,
                                                            __hip_bfloat16* __restrict__ O, int nseq, int G PROF_ARG) {
  extern __shared__ __attribute__((aligned(1024))) char smem[];
  const unsigned lds0 = (unsigned)(size_t)(LDS(char)*)smem;
  const int lane = threadIdx.x & 63;
  const int wid = rfl(threadIdx.x >> 6), half = wid >> 2, wq = wid & 3;
  const int b = blockIdx.x;
  unsigned long long side0 = 0, side1 = 0;
  STAMP(0);
  asm volatile("" :: "s"(Q), "s"(K), "s"(V), "s"(cu), "s"(O), "s"(nseq), "s"(G));
  STAMP(4);
  const int head = 2 * (b & 7) + ((b >> 3) & 1), g = b >> 4;

  const int sa = min(lane >> 3, nseq), sc = min(lane & 7, nseq);
  const int a0 = cu[sa], a1 = cu[min(sa + 1, nseq)], c0 = cu[sc], c1 = cu[min(sc + 1, nseq)];
  if (wid < CODE_PF / 8192) {
    unsigned long long pc;
    asm volatile("s_getpc_b64 %0" : "=s"(pc));
    asm volatile("s_mov_b32 m0, %2\n\ts_nop 0\n\tglobal_load_lds_dword %0, %1"
                 :: "v"(threadIdx.x * 128), "s"(pc & ~127ull), "s"(lds0 + DUMP_OFF) : "memory");
  }
  unsigned voff, vq;
  { const int rr = lane >> 3, dk = lane & 7, drow = 8 * wq + rr;
    voff = drow * ROWB + 16 * (dk ^ (half ? (((drow >> 1) & 1) << 2) : ((drow >> 1) & 7)));
    vq = rr * ROWB + 16 * (dk ^ (rr >> 1));
    asm volatile("" : "+v"(voff), "+v"(vq)); }
  const int la = a1 - a0, lc = c1 - c0;
  asm volatile("" :: "v"(la), "v"(lc));
  STAMP(5);
  const int nb2a = (la + 255) >> 8, nbiga = ((la + 127) >> 7) - nb2a;
  const int nb2c = (lc + 255) >> 8, nbigc = ((lc + 127) >> 7) - nb2c;
  int bef = (8 * lc - (lane & 7) > 8 * la - (lane >> 3)) ? nb2c + (nbigc << 16) : 0;
  int tot = nb2c;
  bef = dpp_add<0xB1>(bef); tot = dpp_add<0xB1>(tot);
  bef = dpp_add<0x4E>(bef); tot = dpp_add<0x4E>(tot);
  bef = dpp_add<0x141>(bef); tot = dpp_add<0x141>(tot);
  const int spare = G - tot, bigbef = bef >> 16;
  const int nsplit = min(max(spare - bigbef, 0), nbiga);
  const int start = (bef & 0xffff) + min(spare, bigbef);
  const unsigned long long hit = __ballot(start <= g && g < start + nb2a + nsplit);
  if (hit == 0) return;
  const int si = __builtin_ctzll(hit);
  const int seq0 = __builtin_amdgcn_readlane(a0, si), L = __builtin_amdgcn_readlane(la, si);
  const int x = g - __builtin_amdgcn_readlane(start, si), ns = __builtin_amdgcn_readlane(nsplit, si);
  const int qrow = x < 2 * ns ? x << 7 : (x - ns) << 8;
  const int nq = min(x < 2 * ns ? 128 : 256, L - qrow);
  const bool split = nq <= 128;
  const int ntile = (L + 31) >> 5;
  const int nA = split ? (ntile + 1) >> 1 : ntile, nB = ntile - nA;
  const int hs = split ? half : 0;
  const int nt = hs ? nB : nA;
  const int nkeys = hs ? L - 32 * nA : min(L, 32 * nA);
  const int qt = split ? wq : wid;

  const char* kvbase = (const char*)(half ? V : K) + head * HEADB;
  const __amdgpu_buffer_rsrc_t rsA = mk_rsrc(kvbase, (unsigned)(seq0 + min(L, 32 * nA) - 1) * ROWB + HEADB);
  const __amdgpu_buffer_rsrc_t rsB = mk_rsrc(kvbase, (unsigned)(seq0 + L - 1) * ROWB + HEADB);
  unsigned sA = (unsigned)seq0 * ROWB;
  const unsigned sAB = (unsigned)nA * TSTEP;
  const unsigned sQ = (unsigned)(seq0 + qrow) * ROWB, qbytes = sQ + (unsigned)(nq - 1) * ROWB + HEADB;
  const __amdgpu_buffer_rsrc_t rsQ = mk_rsrc((const char*)Q + head * HEADB, qbytes);

  const unsigned ddst = lds0 + half * TILEB + wq * 1024, dump = lds0 + DUMP_OFF;
  const unsigned qlds = lds0 + (split ? (wq >> 1) * HALFB + 2 * SLOTB + (wq & 1) * TILEB : 2 * SLOTB + wid * TILEB);
#define TILE_DMA(t, slot) do { DMA3(KVPOL, rsA, voff, sA, (t) < nA ? ddst + (slot) : dump); DMA3(KVPOL, rsB, voff, sA + sAB, (t) < nB ? ddst + (slot) + HALFB : dump); sA += TSTEP; } while (0)
  STAMP(1);
  if (half == 0) TILE_DMA(0, 0);
  {
    const int c = split ? 2 * half : 0;
    const unsigned sq = sQ + (32 * qt + 8 * c) * ROWB;
    DMA3(QPOL, rsQ, vq, sq, qlds + c * 1024);
    DMA3(QPOL, rsQ, vq ^ 64, sq + 8 * ROWB, qlds + c * 1024 + 1024);
    if (!split) { DMA3(QPOL, rsQ, vq, sq + 16 * ROWB, qlds + 2048); DMA3(QPOL, rsQ, vq ^ 64, sq + 24 * ROWB, qlds + 3072); }
  }
  if (half) TILE_DMA(0, 0);
  TILE_DMA(1, SLOTB);

  const unsigned ring = lds0 + hs * HALFB;
  f32x16 o[6];
#pragma unroll
  for (int d = 0; d < 6; ++d) { o[d] = f32x16{}; asm volatile("" : "+v"(o[d])); }
  float mref = -1e30f, lsum = 0.f;
  const bool active = 32 * qt < nq;
  const int ntw = active ? nt : 0;
  unsigned dsl = half ? 2 * SLOTB : 0;
  int td = 3 - half;
#define NEXT_SLOT(x) ((x) == 2 * SLOTB ? 0 : (x) + SLOTB)
  if (half) WAIT_BAR(12); else WAIT_BAR(6);
  STAMP(2);
  if (ntw > 0) {
    const int lane = lane_id(), r32 = lane & 31, hi = lane >> 5;
    const unsigned kb = r32 * 128 + 16 * (hi ^ ((r32 >> 1) & 7));
    const unsigned kbr = ring + kb;
    const unsigned vbase = ring + TILEB + (4 * hi + ((lane >> 2) & 3)) * 128 + 64 * (((lane >> 3) & 1) ^ half) + 32 * ((lane >> 4) & 1) + 8 * (lane & 3);
    const int rem = nkeys - 4 * hi;
    bf16x8 qf[12], kw[4];
    s16x4 va[8];
    f32x16 s, x;
    u32x4 p0, p1;
    unsigned kn, kn1, kn2, kn3, v0, v1;
    unsigned cur = 0, nx1 = SLOTB;
#define ORD(i) ((i) % 6 < 3 ? 2 * ((i) % 6) + 1 : 2 * ((i) % 6 - 3))
#define VRD(i) do { const unsigned a_ = ((ORD(i) & 1) ? v1 : v0) + 4096 * (ORD(i) >> 1) + 2048 * ((i) / 6); \
      va[2 * ((i) & 3)] = vtr(a_); va[2 * ((i) & 3) + 1] = vtr(a_ + 1024); } while (0)
#define VFR(i) __builtin_shufflevector(va[2 * ((i) & 3)], va[2 * ((i) & 3) + 1], 0, 1, 2, 3, 4, 5, 6, 7)
#define KRD(t) kw[(t) & 3] = ldf((((t) & 3) == 0 ? kn : ((t) & 3) == 1 ? kn1 : ((t) & 3) == 2 ? kn2 : kn3) + 4096 * ((t) >> 2))
#define KADDR(slot) do { kn = kbr + (slot); kn1 = kn ^ 32; kn2 = kn ^ 64; kn3 = kn ^ 96; } while (0)
#define VADDR(slot) do { v0 = vbase + (slot); v1 = (vbase ^ 64) + (slot); } while (0)
#define QK(t) s = MFMA(kw[(t) & 3], qf[t], (t) == 0 ? f32x16{} : s)
#define PV(i) o[ORD(i)] = MFMA(VFR(i), __builtin_bit_cast(bf16x8, (i) < 6 ? p0 : p1), o[ORD(i)])
#define EX2(r) do { x[r] = __builtin_amdgcn_exp2f(x[r]); x[(r) + 1] = __builtin_amdgcn_exp2f(x[(r) + 1]); } while (0)
#define AD2(r) do { lsum += x[r]; lsum += x[(r) + 1]; } while (0)
#define CV2(P, i, r) do { P[i] = cvtpk(x[r], x[(r) + 1]); P[(i) + 1] = cvtpk(x[(r) + 2], x[(r) + 3]); asm volatile("" : "+v"(P)); } while (0)
#define FM4(r) do { x[r] = __builtin_fmaf(s[r], CEXP, -mref); x[(r) + 1] = __builtin_fmaf(s[(r) + 1], CEXP, -mref); \
      x[(r) + 2] = __builtin_fmaf(s[(r) + 2], CEXP, -mref); x[(r) + 3] = __builtin_fmaf(s[(r) + 3], CEXP, -mref); } while (0)
#define PIN() do { asm volatile("" : "+v"(x), "+v"(lsum)); SBAR(); } while (0)
#define MX3(a, b, c) __builtin_fmaxf(__builtin_fmaxf((a), (b)), (c))
#define MASK(t) do { if (__builtin_expect(nkeys - 32 * (t) < 32, 0)) { \
      _Pragma("unroll") for (int r = 0; r < 16; ++r) s[r] = ((r & 3) + 8 * (r >> 2) + 32 * (t) < rem) ? s[r] : -INFINITY; } } while (0)
#define ROWMAX(m) do { auto r_ = __builtin_amdgcn_permlane32_swap(__float_as_uint(m), __float_as_uint(m), false, false); \
      m = max2f(__uint_as_float(r_[0]), __uint_as_float(r_[1])) * CEXP; } while (0)
    {
      bf16x8 k0[12];
#pragma unroll
      for (int t = 0; t < 12; ++t) { qf[t] = ldf(((qlds + kb) ^ (32 * (t & 3))) + 4096 * (t >> 2)); k0[t] = ldf((kbr ^ (32 * (t & 3))) + 4096 * (t >> 2)); }
      SBAR();
#pragma unroll
      for (int t = 0; t < 12; ++t) s = MFMA(k0[t], qf[t], t == 0 ? f32x16{} : s);
      KADDR(nx1);
      SBAR();
      if (half) WAIT_BAR(6); else WAIT_BAR(0);
      KRD(0); KRD(1); KRD(2); KRD(3);
      if (half == 0) TILE_DMA(2, 2 * SLOTB);
      MASK(0);
      float mx = MX3(MX3(MX3(s[0], s[1], s[2]), MX3(s[3], s[4], s[5]), MX3(s[6], s[7], s[8])), MX3(s[9], s[10], s[11]), MX3(MX3(s[12], s[13], s[14]), s[15], s[15]));
      ROWMAX(mx); mref = mx;
      FM4(0); FM4(4); FM4(8); FM4(12); PIN();
    }
    STAMP(3);
    for (int j = 1; j < ntw; ++j) {
      const unsigned dd = ddst + dsl;
      const unsigned dA = td < nA ? dd : dump, dB = td < nB ? dd + HALFB : dump, sB = sA + sAB;
      SBAR();
      QK(0);  KRD(4);  EX2(0);           dma1<0>(rsA, voff, sA, dA); PIN();
      QK(1);  KRD(5);  EX2(2);  AD2(0);  PIN();
      QK(2);  KRD(6);  EX2(4);  AD2(2);  dma1<1>(rsA, voff, sA, dA); PIN();
      QK(3);  KRD(7);  EX2(6);  AD2(4);  PIN();
      QK(4);  KRD(8);  EX2(8);  AD2(6);  dma1<2>(rsA, voff, sA, dA); PIN();
      QK(5);  KRD(9);  EX2(10); AD2(8);  PIN();
      QK(6);  KRD(10); EX2(12); AD2(10); dma1<0>(rsB, voff, sB, dB); PIN();
      QK(7);  KRD(11); EX2(14); AD2(12); VADDR(cur); PIN();
      QK(8);  VRD(0);  CV2(p0, 0, 0);  AD2(14); PIN();
      QK(9);  VRD(1);  CV2(p0, 2, 4);  dma1<1>(rsB, voff, sB, dB); SBAR();
      QK(10); VRD(2);  CV2(p1, 0, 8);  SBAR();
      QK(11); VRD(3);  CV2(p1, 2, 12); dma1<2>(rsB, voff, sB, dB); SBAR();
      PV(0); VRD(4); SBAR();
      PV(1); VRD(5); asm volatile("" ::: "memory"); SBAR();
      MASK(j);
      float m0_ = MX3(s[0], s[1], s[2]), m1_ = MX3(s[3], s[4], s[5]), m2_ = MX3(s[6], s[7], s[8]), m3_ = MX3(s[9], s[10], s[11]);
      asm volatile("" : "+v"(m0_), "+v"(m1_), "+v"(m2_), "+v"(m3_)); SBAR();
      PV(2); VRD(6);
      float mx = MX3(MX3(m0_, m1_, m2_), m3_, MX3(MX3(s[12], s[13], s[14]), s[15], s[15]));
      asm volatile("" : "+v"(mx)); SBAR();
      PV(3); VRD(7); asm volatile("" ::: "memory"); SBAR();
      float al = 1.f; bool pend = false;
      if (__builtin_expect(__any(__builtin_fmaf(mx, CEXP, -mref) > THR), 0)) {
        ROWMAX(mx);
        const float mn = max2f(mref, mx);
        al = __builtin_amdgcn_exp2f(mref - mn); mref = mn; lsum *= al; pend = true;
      }
      PV(4); VRD(8);  FM4(0);  PIN();
      PV(5); VRD(9);  FM4(4);  PIN();
      PV(6); VRD(10); FM4(8);  PIN();
      PV(7); VRD(11); FM4(12); PIN();
      cur = nx1; nx1 = NEXT_SLOT(nx1); dsl = NEXT_SLOT(dsl); ++td; sA += TSTEP;
      PV(8); KADDR(nx1); asm volatile("" : "+v"(kn), "+v"(kn1), "+v"(kn2), "+v"(kn3)); SBAR();
      PV(9); SBAR();
      WAIT_BAR(6); SBAR();
      PV(10); SBAR(); KRD(0); KRD(1); KRD(2); KRD(3); asm volatile("" ::: "memory"); SBAR();
      PV(11); SBAR();
      if (__builtin_expect(pend, 0)) {
#pragma unroll
        for (int d = 0; d < 6; ++d) o[d] *= al;
      }
    }
    VADDR(cur);
    VRD(0); VRD(1); VRD(2); VRD(3);
    EX2(0); EX2(2); EX2(4); EX2(6); CV2(p0, 0, 0); CV2(p0, 2, 4); PIN();
    PV(0); VRD(4); EX2(8);  PIN();
    PV(1); VRD(5); EX2(10); PIN();
    PV(2); VRD(6); EX2(12); PIN();
    PV(3); VRD(7); EX2(14); PIN();
    PV(4); VRD(8); CV2(p1, 0, 8); SBAR();
    PV(5); VRD(9); CV2(p1, 2, 12); SBAR();
    PV(6); VRD(10); AD2(0); AD2(2); PIN();
    PV(7); VRD(11); AD2(4); AD2(6); PIN();
    PV(8); AD2(8); AD2(10); PIN();
    PV(9); AD2(12); AD2(14); PIN();
    PV(10); PV(11); SBAR();
    if (ntw < nA) WAIT_BAR(0);
  } else {
    if (half) WAIT_BAR(6); else { WAIT_BAR(0); TILE_DMA(2, 2 * SLOTB); }
    for (int j = 1; j < nA; ++j) {
      TILE_DMA(td, dsl); dsl = NEXT_SLOT(dsl); ++td;
      WAIT_BAR(6);
    }
  }
  STAMP(13);
  lsum = swap_add(lsum);
  const __amdgpu_buffer_rsrc_t rsO = mk_rsrc((char*)O + head * HEADB, qbytes);
  {
  const int lane = lane_id(), r32 = lane & 31, hi = lane >> 5, rr = lane >> 3, dk = lane & 7;
  if (split) {
    const unsigned xo = (wq == 3 ? (unsigned)(nA % 3) * SLOTB + TILEB : wq * SLOTB) + lane * 16;
    const unsigned xw = lds0 + half * HALFB + xo, xr = lds0 + (half ^ 1) * HALFB + xo;
    *(LDS(f32x2)*)(lds0 + ML_OFF + wid * 512 + lane * 8) = f32x2{mref, lsum};
#pragma unroll
    for (int p = 0; p < 3; ++p)
#pragma unroll
      for (int t = 0; t < 4; ++t) {
        const f32x16& y = o[2 * p + 1];
        *(LDS(f32x4)*)(xw + (4 * p + t) * 1024) = f32x4{y[4 * t], y[4 * t + 1], y[4 * t + 2], y[4 * t + 3]};
      }
    asm volatile("s_waitcnt lgkmcnt(0)\n\ts_barrier" ::: "memory");
    if (active) {
      const f32x2 ml = *(const LDS(f32x2)*)(lds0 + ML_OFF + (wid ^ 4) * 512 + lane * 8);
      f32x4 y[12];
#pragma unroll
      for (int i = 0; i < 12; ++i) y[i] = *(const LDS(f32x4)*)(xr + i * 1024);
      SBAR();
      const float ms = max2f(mref, ml[0]);
      float fa = __builtin_amdgcn_exp2f(mref - ms), fb = __builtin_amdgcn_exp2f(ml[0] - ms);
      const float inv = __builtin_amdgcn_rcpf(fa * lsum + fb * ml[1]);
      fa *= inv; fb *= inv;
      u32x2 w[12];
#pragma unroll
      for (int i = 0; i < 12; ++i) {
        const f32x16& z = o[2 * (i >> 2)]; const int t = i & 3;
        w[i] = u32x2{cvtpk(z[4 * t] * fa + y[i][0] * fb, z[4 * t + 1] * fa + y[i][1] * fb), cvtpk(z[4 * t + 2] * fa + y[i][2] * fb, z[4 * t + 3] * fa + y[i][3] * fb)};
      }
      SBAR();
      const unsigned stg = xr - lane * 16;
      const unsigned sw = stg + r32 * 64 + 8 * hi + 16 * ((r32 >> 1) & 3);
#pragma unroll
      for (int i = 0; i < 12; ++i) *(LDS(u32x2)*)((sw ^ (16 * (i & 3))) + (i >> 2) * 2048) = w[i];
      asm volatile("s_waitcnt lgkmcnt(0)" ::: "memory");
      const int sr = lane >> 2, sk = lane & 3;
      const unsigned srd = stg + sr * 64 + 16 * (sk ^ ((sr >> 1) & 3));
      u32x4 z[6];
#pragma unroll
      for (int i = 0; i < 6; ++i) z[i] = *(const LDS(u32x4)*)(srd + i * 1024);
      SBAR();
      const unsigned go = (32 * wq + sr) * ROWB + 64 * half + 16 * sk;
#pragma unroll
      for (int i = 0; i < 6; ++i) __builtin_amdgcn_raw_buffer_store_b128(z[i], rsO, go + (i >> 1) * 128, sQ + (i & 1) * 16 * ROWB, 0);
    }
  } else if (active) {
    const unsigned stg = lds0 + (wid < 6 ? HALFB + wid * TILEB : (wid - 6) * SLOTB);
    const float inv = __builtin_amdgcn_rcpf(lsum);
    u32x2 w[24];
#pragma unroll
    for (int i = 0; i < 24; ++i) { const f32x16& z = o[i >> 2]; const int t = i & 3;
      w[i] = u32x2{cvtpk(z[4 * t] * inv, z[4 * t + 1] * inv), cvtpk(z[4 * t + 2] * inv, z[4 * t + 3] * inv)}; }
    SBAR();
    const unsigned sw = stg + r32 * 128 + 8 * hi + 16 * ((r32 & 7) ^ (4 * half));
#pragma unroll
    for (int i = 0; i < 24; ++i) *(LDS(u32x2)*)((sw ^ (16 * (i & 7))) + (i >> 3) * 4096) = w[i];
    asm volatile("s_waitcnt lgkmcnt(0)" ::: "memory");
    const unsigned srd = stg + rr * 128 + 16 * (dk ^ rr);
    u32x4 z[12];
#pragma unroll
    for (int i = 0; i < 12; ++i) z[i] = *(const LDS(u32x4)*)(srd + i * 1024);
    SBAR();
    const unsigned go = (32 * wid + rr) * ROWB + 16 * dk;
#pragma unroll
    for (int i = 0; i < 12; ++i) __builtin_amdgcn_raw_buffer_store_b128(z[i], rsO, go + (i >> 2) * 128, sQ + (i & 3) * 8 * ROWB, 0);
  }
  }
  STAMP(14);
  asm volatile("s_waitcnt vmcnt(0)" ::: "memory");
  STAMP(15);
  PROF_END();
}

extern "C" void kernel_launch(void* const* d_in, const int* in_sizes, int n_in,
                              void* d_out, int out_size, void* d_ws, size_t ws_size,
                              hipStream_t stream) {
  const int T = in_sizes[0] / (NH * HD), nseq = in_sizes[3] - 1;
  (void)hipFuncSetAttribute((const void*)varlen_attn_kernel, hipFuncAttributeMaxDynamicSharedMemorySize, (int)LDS_BYTES);
  for (int s0 = 0; s0 < nseq; s0 += 8) {
    const int ns = nseq - s0 < 8 ? nseq - s0 : 8;
    int G = T / 256 + ns;
    if (G < 16) G = 16;
    hipLaunchKernelGGL(varlen_attn_kernel, dim3(NH * G), dim3(512), LDS_BYTES, stream,
                       (const __hip_bfloat16*)d_in[0], (const __hip_bfloat16*)d_in[1], (const __hip_bfloat16*)d_in[2],
                       (const int*)d_in[3] + s0, (__hip_bfloat16*)d_out, ns, G PROF_PASS);
  }
}
```

```cpp
#define STAUX 19
#include <hip/hip_runtime.h>
#include <hip/hip_bf16.h>
#include <type_traits>

typedef __attribute__((ext_vector_type(8))) short bf16x8;
typedef __attribute__((ext_vector_type(4))) short s16x4;
typedef __attribute__((ext_vector_type(16))) float f32x16;
typedef __attribute__((ext_vector_type(4))) float f32x4;
typedef __attribute__((ext_vector_type(2))) float f32x2;
typedef __attribute__((ext_vector_type(4))) unsigned u32x4;
typedef __attribute__((ext_vector_type(2))) unsigned u32x2;
typedef __attribute__((ext_vector_type(2))) __bf16 bf16x2;
typedef __attribute__((ext_vector_type(2))) int i32x2;
#define LDS(T) __attribute__((address_space(3))) T
#define DI __device__ __forceinline__
#define SBAR() __builtin_amdgcn_sched_barrier(0)

constexpr int NH = 16, HD = 192;
constexpr unsigned ROWB = NH * HD * 2;
constexpr unsigned HEADB = HD * 2;
constexpr unsigned TILEB = 32 * HEADB;
constexpr unsigned SLOTB = 2 * TILEB;
constexpr unsigned HALFB = 3 * SLOTB;
constexpr unsigned ML_OFF = 2 * HALFB;
constexpr unsigned DUMP_OFF = ML_OFF + 8 * 512;
constexpr unsigned LDS_BYTES = DUMP_OFF + 9216;
constexpr unsigned TSTEP = 32 * ROWB;
constexpr float CEXP = 0.07216878364870322f * 1.4426950408889634f;
constexpr float THR = 8.0f;
#define CODE_PF 8192
#define KVPOL ""
#define QPOL ""

DI unsigned rfl(unsigned x) { return __builtin_amdgcn_readfirstlane(x); }
DI unsigned cvtpk(float lo, float hi) { f32x2 v = {lo, hi}; bf16x2 b = __builtin_convertvector(v, bf16x2); return __builtin_bit_cast(unsigned, b); }
DI float max3f(float a, float b, float c) { float r; asm("v_max3_f32 %0, %1, %2, %3" : "=v"(r) : "v"(a), "v"(b), "v"(c)); return r; }
DI float max2f(float a, float b) { float r; asm("v_max_f32_e32 %0, %1, %2" : "=v"(r) : "v"(a), "v"(b)); return r; }
DI float swap_add(float x) { auto r = __builtin_amdgcn_permlane32_swap(__float_as_uint(x), __float_as_uint(x), false, false); return __uint_as_float(r[0]) + __uint_as_float(r[1]); }
DI __amdgpu_buffer_rsrc_t mk_rsrc(const void* base, unsigned bytes) {
  return __builtin_amdgcn_make_buffer_rsrc((void*)base, (short)0, (int)bytes, 0x00020000);
}
#define DMA3(POL, rs, voff, soff, ldsdst) \
  asm volatile("s_mov_b32 m0, %3\n\ts_nop 0\n\t" \
               "buffer_load_dwordx4 %0, %1, %2 offen " POL " lds\n\t" \
               "s_add_u32 m0, m0, 3968\n\ts_nop 0\n\t" \
               "buffer_load_dwordx4 %0, %1, %2 offen offset:128 " POL " lds\n\t" \
               "s_add_u32 m0, m0, 3968\n\ts_nop 0\n\t" \
               "buffer_load_dwordx4 %0, %1, %2 offen offset:256 " POL " lds" \
               :: "v"(voff), "s"(rs), "s"(soff), "s"(ldsdst) : "memory", "scc")
template <int P> DI void dma1(__amdgpu_buffer_rsrc_t rs, unsigned voff, unsigned soff, unsigned ldsdst) {
  asm volatile("s_add_u32 m0, %3, %4\n\ts_nop 0\n\t"
               "buffer_load_dwordx4 %0, %1, %2 offen offset:%5 " KVPOL " lds"
               :: "v"(voff), "s"(rs), "s"(soff), "s"(ldsdst), "i"(3968 * P), "i"(128 * P) : "memory", "scc");
}
#define WAIT_BAR(N) asm volatile("s_waitcnt vmcnt(" #N ") lgkmcnt(0)\n\ts_barrier" ::: "memory")
DI int lane_id() { int l; asm volatile("v_mbcnt_lo_u32_b32 %0, -1, 0\n\tv_mbcnt_hi_u32_b32 %0, -1, %0" : "=v"(l)); return l; }
#define STAMP(i)
#define PROF_ARG
#define PROF_PASS
#define PROF_END()
#define MFMA(a, b, c) __builtin_amdgcn_mfma_f32_32x32x16_bf16((a), (b), (c), 0, 0, 0)
typedef short v4i16_t __attribute__((ext_vector_type(4)));
DI s16x4 vtr(unsigned a) { return __builtin_bit_cast(s16x4, __builtin_amdgcn_ds_read_tr16_b64_v4i16((LDS(v4i16_t)*)a)); }
DI bf16x8 ldf(unsigned a) { return *(const LDS(bf16x8)*)a; }
template <int CTRL> DI int dpp_add(int x) { return x + __builtin_amdgcn_update_dpp(0, x, CTRL, 0xf, 0xf, true); }

__global__ __launch_bounds__(512, 2) void varlen_attn_kernel(const __hip_bfloat16* __restrict__ Q, const __hip_bfloat16* __restrict__ K,
                                                            const __hip_bfloat16* __restrict__ V, const int* __restrict__ cu,
                                                            __hip_bfloat16* __restrict__ O, int nseq, int G PROF_ARG) {
  extern __shared__ __attribute__((aligned(1024))) char smem[];
  const unsigned lds0 = (unsigned)(size_t)(LDS(char)*)smem;
  const int lane = threadIdx.x & 63;
  const int wid = rfl(threadIdx.x >> 6), half = wid >> 2, wq = wid & 3;
  const int b = blockIdx.x;
  STAMP(0);
  asm volatile("" :: "s"(Q), "s"(K), "s"(V), "s"(cu), "s"(O), "s"(nseq), "s"(G));
  STAMP(4);
  const int head = 2 * (b & 7) + ((b >> 3) & 1), g = b >> 4;

  const int sa = min(lane >> 3, nseq), sc = min(lane & 7, nseq);
  const int a0 = cu[sa], a1 = cu[min(sa + 1, nseq)], c0 = cu[sc], c1 = cu[min(sc + 1, nseq)];
  if (wid < CODE_PF / 8192) {
    unsigned long long pc;
    asm volatile("s_getpc_b64 %0" : "=s"(pc));
    asm volatile("s_mov_b32 m0, %2\n\ts_nop 0\n\tglobal_load_lds_dword %0, %1"
                 :: "v"(threadIdx.x * 128), "s"(pc & ~127ull), "s"(lds0 + DUMP_OFF) : "memory");
  }
  unsigned voff, vq;
  { const int rr = lane >> 3, dk = lane & 7, drow = 8 * wq + rr;
    voff = drow * ROWB + 16 * (dk ^ (half ? (((drow >> 1) & 1) << 2) : ((drow >> 1) & 7)));
    vq = rr * ROWB + 16 * (dk ^ (rr >> 1));
    asm volatile("" : "+v"(voff), "+v"(vq)); }
  const int la = a1 - a0, lc = c1 - c0;
  STAMP(5);
  const int nb2a = (la + 255) >> 8, nbiga = ((la + 127) >> 7) - nb2a;
  const int nb2c = (lc + 255) >> 8, nbigc = ((lc + 127) >> 7) - nb2c;
  int bef = (8 * lc - (lane & 7) > 8 * la - (lane >> 3)) ? nb2c + (nbigc << 16) : 0;
  int tot = nb2c;
  bef = dpp_add<0xB1>(bef); tot = dpp_add<0xB1>(tot);
  bef = dpp_add<0x4E>(bef); tot = dpp_add<0x4E>(tot);
  bef = dpp_add<0x141>(bef); tot = dpp_add<0x141>(tot);
  const int spare = G - tot, bigbef = bef >> 16;
  const int nsplit = min(max(spare - bigbef, 0), nbiga);
  const int start = (bef & 0xffff) + min(spare, bigbef);
  const unsigned long long hit = __ballot(start <= g && g < start + nb2a + nsplit);
  if (hit == 0) return;
  const int si = __builtin_ctzll(hit);
  const int seq0 = __builtin_amdgcn_readlane(a0, si), L = __builtin_amdgcn_readlane(la, si);
  const int x = g - __builtin_amdgcn_readlane(start, si), ns = __builtin_amdgcn_readlane(nsplit, si);
  const int qrow = x < 2 * ns ? x << 7 : (x - ns) << 8;
  const int nq = min(x < 2 * ns ? 128 : 256, L - qrow);
  const bool split = nq <= 128;
  const int ntile = (L + 31) >> 5;
  const int nA = split ? (ntile + 1) >> 1 : ntile, nB = ntile - nA;
  const int hs = split ? half : 0;
  const int nt = hs ? nB : nA;
  const int nkeys = hs ? L - 32 * nA : min(L, 32 * nA);
  const int qt = split ? wq : wid;

  const char* kvbase = (const char*)(half ? V : K) + head * HEADB;
  const __amdgpu_buffer_rsrc_t rsA = mk_rsrc(kvbase, (unsigned)(seq0 + min(L, 32 * nA) - 1) * ROWB + HEADB);
  const __amdgpu_buffer_rsrc_t rsB = mk_rsrc(kvbase, (unsigned)(seq0 + L - 1) * ROWB + HEADB);
  unsigned sA = (unsigned)seq0 * ROWB;
  const unsigned sAB = (unsigned)nA * TSTEP;
  const unsigned sQ = (unsigned)(seq0 + qrow) * ROWB, qbytes = sQ + (unsigned)(nq - 1) * ROWB + HEADB;
  const __amdgpu_buffer_rsrc_t rsQ = mk_rsrc((const char*)Q + head * HEADB, qbytes);

  const unsigned ddst = lds0 + half * TILEB + wq * 1024, dump = lds0 + DUMP_OFF;
  const unsigned qlds = lds0 + (split ? (wq >> 1) * HALFB + 2 * SLOTB + (wq & 1) * TILEB : 2 * SLOTB + wid * TILEB);
#define TILE_DMA(t, slot) do { DMA3(KVPOL, rsA, voff, sA, (t) < nA ? ddst + (slot) : dump); DMA3(KVPOL, rsB, voff, sA + sAB, (t) < nB ? ddst + (slot) + HALFB : dump); sA += TSTEP; } while (0)
  STAMP(1);
  if (half == 0) TILE_DMA(0, 0);
  {
    const int c = split ? 2 * half : 0;
    const unsigned sq = sQ + (32 * qt + 8 * c) * ROWB;
    DMA3(QPOL, rsQ, vq, sq, qlds + c * 1024);
    DMA3(QPOL, rsQ, vq ^ 64, sq + 8 * ROWB, qlds + c * 1024 + 1024);
    if (!split) { DMA3(QPOL, rsQ, vq, sq + 16 * ROWB, qlds + 2048); DMA3(QPOL, rsQ, vq ^ 64, sq + 24 * ROWB, qlds + 3072); }
  }
  if (half) TILE_DMA(0, 0);
  TILE_DMA(1, SLOTB);

  const unsigned ring = lds0 + hs * HALFB;
  f32x16 o[6];
#pragma unroll
  for (int d = 0; d < 6; ++d) { o[d] = f32x16{}; asm volatile("" : "+v"(o[d])); }
  float mref = -1e30f, lsum = 0.f;
  const bool active = 32 * qt < nq;
  const int ntw = active ? nt : 0;
  unsigned dsl = half ? 2 * SLOTB : 0;
  int td = 3 - half;
#define NEXT_SLOT(x) ((x) == 2 * SLOTB ? 0 : (x) + SLOTB)
  if (half) WAIT_BAR(12); else WAIT_BAR(6);
  STAMP(2);
  if (ntw > 0) {
    const int lane = lane_id(), r32 = lane & 31, hi = lane >> 5;
    const unsigned kb = r32 * 128 + 16 * (hi ^ ((r32 >> 1) & 7));
    const unsigned kbr = ring + kb;
    const unsigned vbase = ring + TILEB + (4 * hi + ((lane >> 2) & 3)) * 128 + 64 * (((lane >> 3) & 1) ^ half) + 32 * ((lane >> 4) & 1) + 8 * (lane & 3);
    const int rem = nkeys - 4 * hi;
    bf16x8 qf[12], kw[4];
    s16x4 va[8];
    f32x16 s, x;
    u32x4 p0, p1;
    unsigned kn, kn1, kn2, kn3, v0, v1;
    unsigned cur = 0, nx1 = SLOTB;
#define ORD(i) ((i) % 6 < 3 ? 2 * ((i) % 6) + 1 : 2 * ((i) % 6 - 3))
#define VRD(i) do { const unsigned a_ = ((ORD(i) & 1) ? v1 : v0) + 4096 * (ORD(i) >> 1) + 2048 * ((i) / 6); \
      va[2 * ((i) & 3)] = vtr(a_); va[2 * ((i) & 3) + 1] = vtr(a_ + 1024); } while (0)
#define VFR(i) __builtin_shufflevector(va[2 * ((i) & 3)], va[2 * ((i) & 3) + 1], 0, 1, 2, 3, 4, 5, 6, 7)
#define KRD(t) kw[(t) & 3] = ldf((((t) & 3) == 0 ? kn : ((t) & 3) == 1 ? kn1 : ((t) & 3) == 2 ? kn2 : kn3) + 4096 * ((t) >> 2))
#define KADDR(slot) do { kn = kbr + (slot); kn1 = kn ^ 32; kn2 = kn ^ 64; kn3 = kn ^ 96; } while (0)
#define VADDR(slot) do { v0 = vbase + (slot); v1 = (vbase ^ 64) + (slot); } while (0)
#define QK(t) s = MFMA(kw[(t) & 3], qf[t], (t) == 0 ? f32x16{} : s)
#define PV(i) o[ORD(i)] = MFMA(VFR(i), __builtin_bit_cast(bf16x8, (i) < 6 ? p0 : p1), o[ORD(i)])
#define EX2(r) do { x[r] = __builtin_amdgcn_exp2f(x[r]); x[(r) + 1] = __builtin_amdgcn_exp2f(x[(r) + 1]); } while (0)
#define AD2(r) do { lsum += x[r]; lsum += x[(r) + 1]; } while (0)
#define CV2(P, i, r) do { P[i] = cvtpk(x[r], x[(r) + 1]); P[(i) + 1] = cvtpk(x[(r) + 2], x[(r) + 3]); asm volatile("" : "+v"(P)); } while (0)
#define FM4(r) do { x[r] = __builtin_fmaf(s[r], CEXP, -mref); x[(r) + 1] = __builtin_fmaf(s[(r) + 1], CEXP, -mref); \
      x[(r) + 2] = __builtin_fmaf(s[(r) + 2], CEXP, -mref); x[(r) + 3] = __builtin_fmaf(s[(r) + 3], CEXP, -mref); } while (0)
#define PIN() do { asm volatile("" : "+v"(x), "+v"(lsum)); SBAR(); } while (0)
#define MX3(a, b, c) __builtin_fmaxf(__builtin_fmaxf((a), (b)), (c))
#define MASK(t) do { if (__builtin_expect(nkeys - 32 * (t) < 32, 0)) { \
      _Pragma("unroll") for (int r = 0; r < 16; ++r) s[r] = ((r & 3) + 8 * (r >> 2) + 32 * (t) < rem) ? s[r] : -INFINITY; } } while (0)
#define ROWMAX(m) do { auto r_ = __builtin_amdgcn_permlane32_swap(__float_as_uint(m), __float_as_uint(m), false, false); \
      m = max2f(__uint_as_float(r_[0]), __uint_as_float(r_[1])) * CEXP; } while (0)
    {
      bf16x8 k0[12];
#pragma unroll
      for (int t = 0; t < 12; ++t) { qf[t] = ldf(((qlds + kb) ^ (32 * (t & 3))) + 4096 * (t >> 2)); k0[t] = ldf((kbr ^ (32 * (t & 3))) + 4096 * (t >> 2)); }
      SBAR();
#pragma unroll
      for (int t = 0; t < 12; ++t) s = MFMA(k0[t], qf[t], t == 0 ? f32x16{} : s);
      KADDR(nx1);
      SBAR();
      if (half) WAIT_BAR(6); else WAIT_BAR(0);
      KRD(0); KRD(1); KRD(2); KRD(3);
      if (half == 0) TILE_DMA(2, 2 * SLOTB);
      MASK(0);
      float mx = MX3(MX3(MX3(s[0], s[1], s[2]), MX3(s[3], s[4], s[5]), MX3(s[6], s[7], s[8])), MX3(s[9], s[10], s[11]), MX3(MX3(s[12], s[13], s[14]), s[15], s[15]));
      ROWMAX(mx); mref = mx;
      FM4(0); FM4(4); FM4(8); FM4(12); PIN();
    }
    STAMP(3);
    for (int j = 1; j < ntw; ++j) {
      const unsigned dd = ddst + dsl;
      const unsigned dA = td < nA ? dd : dump, dB = td < nB ? dd + HALFB : dump, sB = sA + sAB;
      SBAR();
      QK(0);  KRD(4);  EX2(0);           dma1<0>(rsA, voff, sA, dA); PIN();
      QK(1);  KRD(5);  EX2(2);  AD2(0);  PIN();
      QK(2);  KRD(6);  EX2(4);  AD2(2);  dma1<1>(rsA, voff, sA, dA); PIN();
      QK(3);  KRD(7);  EX2(6);  AD2(4);  PIN();
      QK(4);  KRD(8);  EX2(8);  AD2(6);  dma1<2>(rsA, voff, sA, dA); PIN();
      QK(5);  KRD(9);  EX2(10); AD2(8);  PIN();
      QK(6);  KRD(10); EX2(12); AD2(10); dma1<0>(rsB, voff, sB, dB); PIN();
      QK(7);  KRD(11); EX2(14); AD2(12); VADDR(cur); PIN();
      QK(8);  VRD(0);  CV2(p0, 0, 0);  AD2(14); PIN();
      QK(9);  VRD(1);  CV2(p0, 2, 4);  dma1<1>(rsB, voff, sB, dB); SBAR();
      QK(10); VRD(2);  CV2(p1, 0, 8);  SBAR();
      QK(11); VRD(3);  CV2(p1, 2, 12); dma1<2>(rsB, voff, sB, dB); SBAR();
      PV(0); VRD(4); SBAR();
      PV(1); VRD(5); asm volatile("" ::: "memory"); SBAR();
      MASK(j);
      float m0_ = MX3(s[0], s[1], s[2]), m1_ = MX3(s[3], s[4], s[5]), m2_ = MX3(s[6], s[7], s[8]), m3_ = MX3(s[9], s[10], s[11]);
      asm volatile("" : "+v"(m0_), "+v"(m1_), "+v"(m2_), "+v"(m3_)); SBAR();
      PV(2); VRD(6);
      float mx = MX3(MX3(m0_, m1_, m2_), m3_, MX3(MX3(s[12], s[13], s[14]), s[15], s[15]));
      asm volatile("" : "+v"(mx)); SBAR();
      PV(3); VRD(7); asm volatile("" ::: "memory"); SBAR();
      float al = 1.f; bool pend = false;
      if (__builtin_expect(__any(__builtin_fmaf(mx, CEXP, -mref) > THR), 0)) {
        ROWMAX(mx);
        const float mn = max2f(mref, mx);
        al = __builtin_amdgcn_exp2f(mref - mn); mref = mn; lsum *= al; pend = true;
      }
      PV(4); VRD(8);  FM4(0);  PIN();
      PV(5); VRD(9);  FM4(4);  PIN();
      PV(6); VRD(10); FM4(8);  PIN();
      PV(7); VRD(11); FM4(12); PIN();
      cur = nx1; nx1 = NEXT_SLOT(nx1); dsl = NEXT_SLOT(dsl); ++td; sA += TSTEP;
      PV(8); KADDR(nx1); asm volatile("" : "+v"(kn), "+v"(kn1), "+v"(kn2), "+v"(kn3)); SBAR();
      PV(9); SBAR();
      WAIT_BAR(6); SBAR();
      PV(10); SBAR(); KRD(0); KRD(1); KRD(2); KRD(3); asm volatile("" ::: "memory"); SBAR();
      PV(11); SBAR();
      if (__builtin_expect(pend, 0)) {
#pragma unroll
        for (int d = 0; d < 6; ++d) o[d] *= al;
      }
    }
    VADDR(cur);
    VRD(0); VRD(1); VRD(2); VRD(3);
    EX2(0); EX2(2); EX2(4); EX2(6); CV2(p0, 0, 0); CV2(p0, 2, 4); PIN();
    PV(0); VRD(4); EX2(8);  PIN();
    PV(1); VRD(5); EX2(10); PIN();
    PV(2); VRD(6); EX2(12); PIN();
    PV(3); VRD(7); EX2(14); PIN();
    PV(4); VRD(8); CV2(p1, 0, 8); SBAR();
    PV(5); VRD(9); CV2(p1, 2, 12); SBAR();
    PV(6); VRD(10); AD2(0); AD2(2); PIN();
    PV(7); VRD(11); AD2(4); AD2(6); PIN();
    PV(8); AD2(8); AD2(10); PIN();
    PV(9); AD2(12); AD2(14); PIN();
    PV(10); PV(11); SBAR();
    if (ntw < nA) WAIT_BAR(0);
  } else {
    if (half) WAIT_BAR(6); else { WAIT_BAR(0); TILE_DMA(2, 2 * SLOTB); }
    for (int j = 1; j < nA; ++j) {
      TILE_DMA(td, dsl); dsl = NEXT_SLOT(dsl); ++td;
      WAIT_BAR(6);
    }
  }
  STAMP(13);
  lsum = swap_add(lsum);
  const __amdgpu_buffer_rsrc_t rsO = mk_rsrc((char*)O + head * HEADB, qbytes);
  {
  const int lane = lane_id(), r32 = lane & 31, hi = lane >> 5, rr = lane >> 3, dk = lane & 7;
  if (split) {
    const unsigned xo = (wq == 3 ? (unsigned)(nA % 3) * SLOTB + TILEB : wq * SLOTB) + lane * 16;
    const unsigned xw = lds0 + half * HALFB + xo, xr = lds0 + (half ^ 1) * HALFB + xo;
    *(LDS(f32x2)*)(lds0 + ML_OFF + wid * 512 + lane * 8) = f32x2{mref, lsum};
#pragma unroll
    for (int p = 0; p < 3; ++p)
#pragma unroll
      for (int t = 0; t < 4; ++t) {
        const f32x16& y = o[2 * p + 1];
        *(LDS(f32x4)*)(xw + (4 * p + t) * 1024) = f32x4{y[4 * t], y[4 * t + 1], y[4 * t + 2], y[4 * t + 3]};
      }
    asm volatile("s_waitcnt lgkmcnt(0)\n\ts_barrier" ::: "memory");
    if (active) {
      const f32x2 ml = *(const LDS(f32x2)*)(lds0 + ML_OFF + (wid ^ 4) * 512 + lane * 8);
      f32x4 y[12];
#pragma unroll
      for (int i = 0; i < 12; ++i) y[i] = *(const LDS(f32x4)*)(xr + i * 1024);
      SBAR();
      const float ms = max2f(mref, ml[0]);
      float fa = __builtin_amdgcn_exp2f(mref - ms), fb = __builtin_amdgcn_exp2f(ml[0] - ms);
      const float inv = __builtin_amdgcn_rcpf(fa * lsum + fb * ml[1]);
      fa *= inv; fb *= inv;
      u32x2 w[12];
#pragma unroll
      for (int i = 0; i < 12; ++i) {
        const f32x16& z = o[2 * (i >> 2)]; const int t = i & 3;
        w[i] = u32x2{cvtpk(z[4 * t] * fa + y[i][0] * fb, z[4 * t + 1] * fa + y[i][1] * fb), cvtpk(z[4 * t + 2] * fa + y[i][2] * fb, z[4 * t + 3] * fa + y[i][3] * fb)};
      }
      SBAR();
      const unsigned stg = xr - lane * 16;
      const unsigned sw = stg + r32 * 64 + 8 * hi + 16 * ((r32 >> 1) & 3);
#pragma unroll
      for (int i = 0; i < 12; ++i) *(LDS(u32x2)*)((sw ^ (16 * (i & 3))) + (i >> 2) * 2048) = w[i];
      asm volatile("s_waitcnt lgkmcnt(0)" ::: "memory");
      const int sr = lane >> 2, sk = lane & 3;
      const unsigned srd = stg + sr * 64 + 16 * (sk ^ ((sr >> 1) & 3));
      u32x4 z[6];
#pragma unroll
      for (int i = 0; i < 6; ++i) z[i] = *(const LDS(u32x4)*)(srd + i * 1024);
      SBAR();
      const unsigned go = (32 * wq + sr) * ROWB + 64 * half + 16 * sk;
#pragma unroll
      for (int i = 0; i < 6; ++i) __builtin_amdgcn_raw_buffer_store_b128(z[i], rsO, go + (i >> 1) * 128, sQ + (i & 1) * 16 * ROWB, STAUX);
    }
  } else if (active) {
    const unsigned stg = lds0 + (wid < 6 ? HALFB + wid * TILEB : (wid - 6) * SLOTB);
    const float inv = __builtin_amdgcn_rcpf(lsum);
    u32x2 w[24];
#pragma unroll
    for (int i = 0; i < 24; ++i) { const f32x16& z = o[i >> 2]; const int t = i & 3;
      w[i] = u32x2{cvtpk(z[4 * t] * inv, z[4 * t + 1] * inv), cvtpk(z[4 * t + 2] * inv, z[4 * t + 3] * inv)}; }
    SBAR();
    const unsigned sw = stg + r32 * 128 + 8 * hi + 16 * ((r32 & 7) ^ (4 * half));
#pragma unroll
    for (int i = 0; i < 24; ++i) *(LDS(u32x2)*)((sw ^ (16 * (i & 7))) + (i >> 3) * 4096) = w[i];
    asm volatile("s_waitcnt lgkmcnt(0)" ::: "memory");
    const unsigned srd = stg + rr * 128 + 16 * (dk ^ rr);
    u32x4 z[12];
#pragma unroll
    for (int i = 0; i < 12; ++i) z[i] = *(const LDS(u32x4)*)(srd + i * 1024);
    SBAR();
    const unsigned go = (32 * wid + rr) * ROWB + 16 * dk;
#pragma unroll
    for (int i = 0; i < 12; ++i) __builtin_amdgcn_raw_buffer_store_b128(z[i], rsO, go + (i >> 2) * 128, sQ + (i & 3) * 8 * ROWB, STAUX);
  }
  }
  STAMP(14);
  STAMP(15);
  PROF_END();
}

extern "C" void kernel_launch(void* const* d_in, const int* in_sizes, int n_in,
                              void* d_out, int out_size, void* d_ws, size_t ws_size,
                              hipStream_t stream) {
  const int T = in_sizes[0] / (NH * HD), nseq = in_sizes[3] - 1;
  (void)hipFuncSetAttribute((const void*)varlen_attn_kernel, hipFuncAttributeMaxDynamicSharedMemorySize, (int)LDS_BYTES);
  for (int s0 = 0; s0 < nseq; s0 += 8) {
    const int ns = nseq - s0 < 8 ? nseq - s0 : 8;
    int G = T / 256 + ns;
    if (G < 16) G = 16;
    hipLaunchKernelGGL(varlen_attn_kernel, dim3(NH * G), dim3(512), LDS_BYTES, stream,
                       (const __hip_bfloat16*)d_in[0], (const __hip_bfloat16*)d_in[1], (const __hip_bfloat16*)d_in[2],
                       (const int*)d_in[3] + s0, (__hip_bfloat16*)d_out, ns, G PROF_PASS);
  }
}
```

```cpp
#define STAUX 17
#define SIDE 0
#define SIDE_COND (blockIdx.x == 0 && wid == 0)
#define USE_K0 1
#define K0GRID 256
#include <hip/hip_runtime.h>
#include <hip/hip_bf16.h>
#include <type_traits>

typedef __attribute__((ext_vector_type(8))) short bf16x8;
typedef __attribute__((ext_vector_type(4))) short s16x4;
typedef __attribute__((ext_vector_type(16))) float f32x16;
typedef __attribute__((ext_vector_type(4))) float f32x4;
typedef __attribute__((ext_vector_type(2))) float f32x2;
typedef __attribute__((ext_vector_type(4))) unsigned u32x4;
typedef __attribute__((ext_vector_type(2))) unsigned u32x2;
typedef __attribute__((ext_vector_type(2))) __bf16 bf16x2;
typedef __attribute__((ext_vector_type(2))) int i32x2;
#define LDS(T) __attribute__((address_space(3))) T
#define DI __device__ __forceinline__
#define SBAR() __builtin_amdgcn_sched_barrier(0)

constexpr int NH = 16, HD = 192;
constexpr unsigned ROWB = NH * HD * 2;
constexpr unsigned HEADB = HD * 2;
constexpr unsigned TILEB = 32 * HEADB;
constexpr unsigned SLOTB = 2 * TILEB;
constexpr unsigned HALFB = 3 * SLOTB;
constexpr unsigned ML_OFF = 2 * HALFB;
constexpr unsigned DUMP_OFF = ML_OFF + 8 * 512;
constexpr unsigned LDS_BYTES = DUMP_OFF + 9216;
constexpr unsigned TSTEP = 32 * ROWB;
constexpr float CEXP = 0.07216878364870322f * 1.4426950408889634f;
constexpr float THR = 8.0f;
#define CODE_PF 8192
#define KVPOL ""
#define QPOL ""

DI unsigned rfl(unsigned x) { return __builtin_amdgcn_readfirstlane(x); }
DI unsigned cvtpk(float lo, float hi) { f32x2 v = {lo, hi}; bf16x2 b = __builtin_convertvector(v, bf16x2); return __builtin_bit_cast(unsigned, b); }
DI float max3f(float a, float b, float c) { float r; asm("v_max3_f32 %0, %1, %2, %3" : "=v"(r) : "v"(a), "v"(b), "v"(c)); return r; }
DI float max2f(float a, float b) { float r; asm("v_max_f32_e32 %0, %1, %2" : "=v"(r) : "v"(a), "v"(b)); return r; }
DI float swap_add(float x) { auto r = __builtin_amdgcn_permlane32_swap(__float_as_uint(x), __float_as_uint(x), false, false); return __uint_as_float(r[0]) + __uint_as_float(r[1]); }
DI __amdgpu_buffer_rsrc_t mk_rsrc(const void* base, unsigned bytes) {
  return __builtin_amdgcn_make_buffer_rsrc((void*)base, (short)0, (int)bytes, 0x00020000);
}
#define DMA3(POL, rs, voff, soff, ldsdst) \
  asm volatile("s_mov_b32 m0, %3\n\ts_nop 0\n\t" \
               "buffer_load_dwordx4 %0, %1, %2 offen " POL " lds\n\t" \
               "s_add_u32 m0, m0, 3968\n\ts_nop 0\n\t" \
               "buffer_load_dwordx4 %0, %1, %2 offen offset:128 " POL " lds\n\t" \
               "s_add_u32 m0, m0, 3968\n\ts_nop 0\n\t" \
               "buffer_load_dwordx4 %0, %1, %2 offen offset:256 " POL " lds" \
               :: "v"(voff), "s"(rs), "s"(soff), "s"(ldsdst) : "memory", "scc")
template <int P> DI void dma1(__amdgpu_buffer_rsrc_t rs, unsigned voff, unsigned soff, unsigned ldsdst) {
  asm volatile("s_add_u32 m0, %3, %4\n\ts_nop 0\n\t"
               "buffer_load_dwordx4 %0, %1, %2 offen offset:%5 " KVPOL " lds"
               :: "v"(voff), "s"(rs), "s"(soff), "s"(ldsdst), "i"(3968 * P), "i"(128 * P) : "memory", "scc");
}
#define WAIT_BAR(N) asm volatile("s_waitcnt vmcnt(" #N ") lgkmcnt(0)\n\ts_barrier" ::: "memory")
DI int lane_id() { int l; asm volatile("v_mbcnt_lo_u32_b32 %0, -1, 0\n\tv_mbcnt_hi_u32_b32 %0, -1, %0" : "=v"(l)); return l; }
#define STAMP(i) do { if ((i) == 0 || (i) == SIDE) { unsigned long long t_; asm volatile("s_memrealtime %0\n\ts_waitcnt lgkmcnt(0)" : "=s"(t_) :: "memory"); if ((i) == 0) side0 = t_; side1 = t_; } } while (0)
#define PROF_ARG , unsigned long long* prof
#define PROF_PASS , (unsigned long long*)d_ws
#define PROF_END() do { if (SIDE_COND) { const unsigned long long tgt = (USE_K0 ? __hip_atomic_load(prof, __ATOMIC_RELAXED, __HIP_MEMORY_SCOPE_AGENT) : side0) + 4000; while (__builtin_amdgcn_s_memrealtime() < tgt) __builtin_amdgcn_s_sleep(4); } } while (0)
#define MFMA(a, b, c) __builtin_amdgcn_mfma_f32_32x32x16_bf16((a), (b), (c), 0, 0, 0)
typedef short v4i16_t __attribute__((ext_vector_type(4)));
DI s16x4 vtr(unsigned a) { return __builtin_bit_cast(s16x4, __builtin_amdgcn_ds_read_tr16_b64_v4i16((LDS(v4i16_t)*)a)); }
DI bf16x8 ldf(unsigned a) { return *(const LDS(bf16x8)*)a; }
template <int CTRL> DI int dpp_add(int x) { return x + __builtin_amdgcn_update_dpp(0, x, CTRL, 0xf, 0xf, true); }

__global__ __launch_bounds__(512, 2) void varlen_attn_kernel(const __hip_bfloat16* __restrict__ Q, const __hip_bfloat16* __restrict__ K,
                                                            const __hip_bfloat16* __restrict__ V, const int* __restrict__ cu,
                                                            __hip_bfloat16* __restrict__ O, int nseq, int G PROF_ARG) {
  extern __shared__ __attribute__((aligned(1024))) char smem[];
  const unsigned lds0 = (unsigned)(size_t)(LDS(char)*)smem;
  const int lane = threadIdx.x & 63;
  const int wid = rfl(threadIdx.x >> 6), half = wid >> 2, wq = wid & 3;
  const int b = blockIdx.x;
  unsigned long long side0 = 0, side1 = 0;
  STAMP(0);
  asm volatile("" :: "s"(Q), "s"(K), "s"(V), "s"(cu), "s"(O), "s"(nseq), "s"(G));
  STAMP(4);
  const int head = 2 * (b & 7) + ((b >> 3) & 1), g = b >> 4;

  const int sa = min(lane >> 3, nseq), sc = min(lane & 7, nseq);
  const int a0 = cu[sa], a1 = cu[min(sa + 1, nseq)], c0 = cu[sc], c1 = cu[min(sc + 1, nseq)];
  if (wid < CODE_PF / 8192) {
    unsigned long long pc;
    asm volatile("s_getpc_b64 %0" : "=s"(pc));
    asm volatile("s_mov_b32 m0, %2\n\ts_nop 0\n\tglobal_load_lds_dword %0, %1"
                 :: "v"(threadIdx.x * 128), "s"(pc & ~127ull), "s"(lds0 + DUMP_OFF) : "memory");
  }
  unsigned voff, vq;
  { const int rr = lane >> 3, dk = lane & 7, drow = 8 * wq + rr;
    voff = drow * ROWB + 16 * (dk ^ (half ? (((drow >> 1) & 1) << 2) : ((drow >> 1) & 7)));
    vq = rr * ROWB + 16 * (dk ^ (rr >> 1));
    asm volatile("" : "+v"(voff), "+v"(vq)); }
  const int la = a1 - a0, lc = c1 - c0;
  asm volatile("" :: "v"(la), "v"(lc));
  STAMP(5);
  const int nb2a = (la + 255) >> 8, nbiga = ((la + 127) >> 7) - nb2a;
  const int nb2c = (lc + 255) >> 8, nbigc = ((lc + 127) >> 7) - nb2c;
  int bef = (8 * lc - (lane & 7) > 8 * la - (lane >> 3)) ? nb2c + (nbigc << 16) : 0;
  int tot = nb2c;
  bef = dpp_add<0xB1>(bef); tot = dpp_add<0xB1>(tot);
  bef = dpp_add<0x4E>(bef); tot = dpp_add<0x4E>(tot);
  bef = dpp_add<0x141>(bef); tot = dpp_add<0x141>(tot);
  const int spare = G - tot, bigbef = bef >> 16;
  const int nsplit = min(max(spare - bigbef, 0), nbiga);
  const int start = (bef & 0xffff) + min(spare, bigbef);
  const unsigned long long hit = __ballot(start <= g && g < start + nb2a + nsplit);
  if (hit == 0) return;
  const int si = __builtin_ctzll(hit);
  const int seq0 = __builtin_amdgcn_readlane(a0, si), L = __builtin_amdgcn_readlane(la, si);
  const int x = g - __builtin_amdgcn_readlane(start, si), ns = __builtin_amdgcn_readlane(nsplit, si);
  const int qrow = x < 2 * ns ? x << 7 : (x - ns) << 8;
  const int nq = min(x < 2 * ns ? 128 : 256, L - qrow);
  const bool split = nq <= 128;
  const int ntile = (L + 31) >> 5;
  const int nA = split ? (ntile + 1) >> 1 : ntile, nB = ntile - nA;
  const int hs = split ? half : 0;
  const int nt = hs ? nB : nA;
  const int nkeys = hs ? L - 32 * nA : min(L, 32 * nA);
  const int qt = split ? wq : wid;

  const char* kvbase = (const char*)(half ? V : K) + head * HEADB;
  const __amdgpu_buffer_rsrc_t rsA = mk_rsrc(kvbase, (unsigned)(seq0 + min(L, 32 * nA) - 1) * ROWB + HEADB);
  const __amdgpu_buffer_rsrc_t rsB = mk_rsrc(kvbase, (unsigned)(seq0 + L - 1) * ROWB + HEADB);
  unsigned sA = (unsigned)seq0 * ROWB;
  const unsigned sAB = (unsigned)nA * TSTEP;
  const unsigned sQ = (unsigned)(seq0 + qrow) * ROWB, qbytes = sQ + (unsigned)(nq - 1) * ROWB + HEADB;
  const __amdgpu_buffer_rsrc_t rsQ = mk_rsrc((const char*)Q + head * HEADB, qbytes);

  const unsigned ddst = lds0 + half * TILEB + wq * 1024, dump = lds0 + DUMP_OFF;
  const unsigned qlds = lds0 + (split ? (wq >> 1) * HALFB + 2 * SLOTB + (wq & 1) * TILEB : 2 * SLOTB + wid * TILEB);
#define TILE_DMA(t, slot) do { DMA3(KVPOL, rsA, voff, sA, (t) < nA ? ddst + (slot) : dump); DMA3(KVPOL, rsB, voff, sA + sAB, (t) < nB ? ddst + (slot) + HALFB : dump); sA += TSTEP; } while (0)
  STAMP(1);
  if (half == 0) TILE_DMA(0, 0);
  {
    const int c = split ? 2 * half : 0;
    const unsigned sq = sQ + (32 * qt + 8 * c) * ROWB;
    DMA3(QPOL, rsQ, vq, sq, qlds + c * 1024);
    DMA3(QPOL, rsQ, vq ^ 64, sq + 8 * ROWB, qlds + c * 1024 + 1024);
    if (!split) { DMA3(QPOL, rsQ, vq, sq + 16 * ROWB, qlds + 2048); DMA3(QPOL, rsQ, vq ^ 64, sq + 24 * ROWB, qlds + 3072); }
  }
  if (half) TILE_DMA(0, 0);
  TILE_DMA(1, SLOTB);

  const unsigned ring = lds0 + hs * HALFB;
  f32x16 o[6];
#pragma unroll
  for (int d = 0; d < 6; ++d) { o[d] = f32x16{}; asm volatile("" : "+v"(o[d])); }
  float mref = -1e30f, lsum = 0.f;
  const bool active = 32 * qt < nq;
  const int ntw = active ? nt : 0;
  unsigned dsl = half ? 2 * SLOTB : 0;
  int td = 3 - half;
#define NEXT_SLOT(x) ((x) == 2 * SLOTB ? 0 : (x) + SLOTB)
  if (half) WAIT_BAR(12); else WAIT_BAR(6);
  STAMP(2);
  if (ntw > 0) {
    const int lane = lane_id(), r32 = lane & 31, hi = lane >> 5;
    const unsigned kb = r32 * 128 + 16 * (hi ^ ((r32 >> 1) & 7));
    const unsigned kbr = ring + kb;
    const unsigned vbase = ring + TILEB + (4 * hi + ((lane >> 2) & 3)) * 128 + 64 * (((lane >> 3) & 1) ^ half) + 32 * ((lane >> 4) & 1) + 8 * (lane & 3);
    const int rem = nkeys - 4 * hi;
    bf16x8 qf[12], kw[4];
    s16x4 va[8];
    f32x16 s, x;
    u32x4 p0, p1;
    unsigned kn, kn1, kn2, kn3, v0, v1;
    unsigned cur = 0, nx1 = SLOTB;
#define ORD(i) ((i) % 6 < 3 ? 2 * ((i) % 6) + 1 : 2 * ((i) % 6 - 3))
#define VRD(i) do { const unsigned a_ = ((ORD(i) & 1) ? v1 : v0) + 4096 * (ORD(i) >> 1) + 2048 * ((i) / 6); \
      va[2 * ((i) & 3)] = vtr(a_); va[2 * ((i) & 3) + 1] = vtr(a_ + 1024); } while (0)
#define VFR(i) __builtin_shufflevector(va[2 * ((i) & 3)], va[2 * ((i) & 3) + 1], 0, 1, 2, 3, 4, 5, 6, 7)
#define KRD(t) kw[(t) & 3] = ldf((((t) & 3) == 0 ? kn : ((t) & 3) == 1 ? kn1 : ((t) & 3) == 2 ? kn2 : kn3) + 4096 * ((t) >> 2))
#define KADDR(slot) do { kn = kbr + (slot); kn1 = kn ^ 32; kn2 = kn ^ 64; kn3 = kn ^ 96; } while (0)
#define VADDR(slot) do { v0 = vbase + (slot); v1 = (vbase ^ 64) + (slot); } while (0)
#define QK(t) s = MFMA(kw[(t) & 3], qf[t], (t) == 0 ? f32x16{} : s)
#define PV(i) o[ORD(i)] = MFMA(VFR(i), __builtin_bit_cast(bf16x8, (i) < 6 ? p0 : p1), o[ORD(i)])
#define EX2(r) do { x[r] = __builtin_amdgcn_exp2f(x[r]); x[(r) + 1] = __builtin_amdgcn_exp2f(x[(r) + 1]); } while (0)
#define AD2(r) do { lsum += x[r]; lsum += x[(r) + 1]; } while (0)
#define CV2(P, i, r) do { P[i] = cvtpk(x[r], x[(r) + 1]); P[(i) + 1] = cvtpk(x[(r) + 2], x[(r) + 3]); asm volatile("" : "+v"(P)); } while (0)
#define FM4(r) do { x[r] = __builtin_fmaf(s[r], CEXP, -mref); x[(r) + 1] = __builtin_fmaf(s[(r) + 1], CEXP, -mref); \
      x[(r) + 2] = __builtin_fmaf(s[(r) + 2], CEXP, -mref); x[(r) + 3] = __builtin_fmaf(s[(r) + 3], CEXP, -mref); } while (0)
#define PIN() do { asm volatile("" : "+v"(x), "+v"(lsum)); SBAR(); } while (0)
#define MX3(a, b, c) __builtin_fmaxf(__builtin_fmaxf((a), (b)), (c))
#define MASK(t) do { if (__builtin_expect(nkeys - 32 * (t) < 32, 0)) { \
      _Pragma("unroll") for (int r = 0; r < 16; ++r) s[r] = ((r & 3) + 8 * (r >> 2) + 32 * (t) < rem) ? s[r] : -INFINITY; } } while (0)
#define ROWMAX(m) do { auto r_ = __builtin_amdgcn_permlane32_swap(__float_as_uint(m), __float_as_uint(m), false, false); \
      m = max2f(__uint_as_float(r_[0]), __uint_as_float(r_[1])) * CEXP; } while (0)
    {
      bf16x8 k0[12];
#pragma unroll
      for (int t = 0; t < 12; ++t) { qf[t] = ldf(((qlds + kb) ^ (32 * (t & 3))) + 4096 * (t >> 2)); k0[t] = ldf((kbr ^ (32 * (t & 3))) + 4096 * (t >> 2)); }
      SBAR();
#pragma unroll
      for (int t = 0; t < 12; ++t) s = MFMA(k0[t], qf[t], t == 0 ? f32x16{} : s);
      KADDR(nx1);
      SBAR();
      if (half) WAIT_BAR(6); else WAIT_BAR(0);
      KRD(0); KRD(1); KRD(2); KRD(3);
      if (half == 0) TILE_DMA(2, 2 * SLOTB);
      MASK(0);
      float mx = MX3(MX3(MX3(s[0], s[1], s[2]), MX3(s[3], s[4], s[5]), MX3(s[6], s[7], s[8])), MX3(s[9], s[10], s[11]), MX3(MX3(s[12], s[13], s[14]), s[15], s[15]));
      ROWMAX(mx); mref = mx;
      FM4(0); FM4(4); FM4(8); FM4(12); PIN();
    }
    STAMP(3);
    for (int j = 1; j < ntw; ++j) {
      const unsigned dd = ddst + dsl;
      const unsigned dA = td < nA ? dd : dump, dB = td < nB ? dd + HALFB : dump, sB = sA + sAB;
      SBAR();
      QK(0);  KRD(4);  EX2(0);           dma1<0>(rsA, voff, sA, dA); PIN();
      QK(1);  KRD(5);  EX2(2);  AD2(0);  PIN();
      QK(2);  KRD(6);  EX2(4);  AD2(2);  dma1<1>(rsA, voff, sA, dA); PIN();
      QK(3);  KRD(7);  EX2(6);  AD2(4);  PIN();
      QK(4);  KRD(8);  EX2(8);  AD2(6);  dma1<2>(rsA, voff, sA, dA); PIN();
      QK(5);  KRD(9);  EX2(10); AD2(8);  PIN();
      QK(6);  KRD(10); EX2(12); AD2(10); dma1<0>(rsB, voff, sB, dB); PIN();
      QK(7);  KRD(11); EX2(14); AD2(12); VADDR(cur); PIN();
      QK(8);  VRD(0);  CV2(p0, 0, 0);  AD2(14); PIN();
      QK(9);  VRD(1);  CV2(p0, 2, 4);  dma1<1>(rsB, voff, sB, dB); SBAR();
      QK(10); VRD(2);  CV2(p1, 0, 8);  SBAR();
      QK(11); VRD(3);  CV2(p1, 2, 12); dma1<2>(rsB, voff, sB, dB); SBAR();
      PV(0); VRD(4); SBAR();
      PV(1); VRD(5); asm volatile("" ::: "memory"); SBAR();
      MASK(j);
      float m0_ = MX3(s[0], s[1], s[2]), m1_ = MX3(s[3], s[4], s[5]), m2_ = MX3(s[6], s[7], s[8]), m3_ = MX3(s[9], s[10], s[11]);
      asm volatile("" : "+v"(m0_), "+v"(m1_), "+v"(m2_), "+v"(m3_)); SBAR();
      PV(2); VRD(6);
      float mx = MX3(MX3(m0_, m1_, m2_), m3_, MX3(MX3(s[12], s[13], s[14]), s[15], s[15]));
      asm volatile("" : "+v"(mx)); SBAR();
      PV(3); VRD(7); asm volatile("" ::: "memory"); SBAR();
      float al = 1.f; bool pend = false;
      if (__builtin_expect(__any(__builtin_fmaf(mx, CEXP, -mref) > THR), 0)) {
        ROWMAX(mx);
        const float mn = max2f(mref, mx);
        al = __builtin_amdgcn_exp2f(mref - mn); mref = mn; lsum *= al; pend = true;
      }
      PV(4); VRD(8);  FM4(0);  PIN();
      PV(5); VRD(9);  FM4(4);  PIN();
      PV(6); VRD(10); FM4(8);  PIN();
      PV(7); VRD(11); FM4(12); PIN();
      cur = nx1; nx1 = NEXT_SLOT(nx1); dsl = NEXT_SLOT(dsl); ++td; sA += TSTEP;
      PV(8); KADDR(nx1); asm volatile("" : "+v"(kn), "+v"(kn1), "+v"(kn2), "+v"(kn3)); SBAR();
      PV(9); SBAR();
      WAIT_BAR(6); SBAR();
      PV(10); SBAR(); KRD(0); KRD(1); KRD(2); KRD(3); asm volatile("" ::: "memory"); SBAR();
      PV(11); SBAR();
      if (__builtin_expect(pend, 0)) {
#pragma unroll
        for (int d = 0; d < 6; ++d) o[d] *= al;
      }
    }
    VADDR(cur);
    VRD(0); VRD(1); VRD(2); VRD(3);
    EX2(0); EX2(2); EX2(4); EX2(6); CV2(p0, 0, 0); CV2(p0, 2, 4); PIN();
    PV(0); VRD(4); EX2(8);  PIN();
    PV(1); VRD(5); EX2(10); PIN();
    PV(2); VRD(6); EX2(12); PIN();
    PV(3); VRD(7); EX2(14); PIN();
    PV(4); VRD(8); CV2(p1, 0, 8); SBAR();
    PV(5); VRD(9); CV2(p1, 2, 12); SBAR();
    PV(6); VRD(10); AD2(0); AD2(2); PIN();
    PV(7); VRD(11); AD2(4); AD2(6); PIN();
    PV(8); AD2(8); AD2(10); PIN();
    PV(9); AD2(12); AD2(14); PIN();
    PV(10); PV(11); SBAR();
    if (ntw < nA) WAIT_BAR(0);
  } else {
    if (half) WAIT_BAR(6); else { WAIT_BAR(0); TILE_DMA(2, 2 * SLOTB); }
    for (int j = 1; j < nA; ++j) {
      TILE_DMA(td, dsl); dsl = NEXT_SLOT(dsl); ++td;
      WAIT_BAR(6);
    }
  }
  STAMP(13);
  lsum = swap_add(lsum);
  const __amdgpu_buffer_rsrc_t rsO = mk_rsrc((char*)O + head * HEADB, qbytes);
  {
  const int lane = lane_id(), r32 = lane & 31, hi = lane >> 5, rr = lane >> 3, dk = lane & 7;
  if (split) {
    const unsigned xo = (wq == 3 ? (unsigned)(nA % 3) * SLOTB + TILEB : wq * SLOTB) + lane * 16;
    const unsigned xw = lds0 + half * HALFB + xo, xr = lds0 + (half ^ 1) * HALFB + xo;
    *(LDS(f32x2)*)(lds0 + ML_OFF + wid * 512 + lane * 8) = f32x2{mref, lsum};
#pragma unroll
    for (int p = 0; p < 3; ++p)
#pragma unroll
      for (int t = 0; t < 4; ++t) {
        const f32x16& y = o[2 * p + 1];
        *(LDS(f32x4)*)(xw + (4 * p + t) * 1024) = f32x4{y[4 * t], y[4 * t + 1], y[4 * t + 2], y[4 * t + 3]};
      }
    asm volatile("s_waitcnt lgkmcnt(0)\n\ts_barrier" ::: "memory");
    if (active) {
      const f32x2 ml = *(const LDS(f32x2)*)(lds0 + ML_OFF + (wid ^ 4) * 512 + lane * 8);
      f32x4 y[12];
#pragma unroll
      for (int i = 0; i < 12; ++i) y[i] = *(const LDS(f32x4)*)(xr + i * 1024);
      SBAR();
      const float ms = max2f(mref, ml[0]);
      float fa = __builtin_amdgcn_exp2f(mref - ms), fb = __builtin_amdgcn_exp2f(ml[0] - ms);
      const float inv = __builtin_amdgcn_rcpf(fa * lsum + fb * ml[1]);
      fa *= inv; fb *= inv;
      u32x2 w[12];
#pragma unroll
      for (int i = 0; i < 12; ++i) {
        const f32x16& z = o[2 * (i >> 2)]; const int t = i & 3;
        w[i] = u32x2{cvtpk(z[4 * t] * fa + y[i][0] * fb, z[4 * t + 1] * fa + y[i][1] * fb), cvtpk(z[4 * t + 2] * fa + y[i][2] * fb, z[4 * t + 3] * fa + y[i][3] * fb)};
      }
      SBAR();
      const unsigned stg = xr - lane * 16;
      const unsigned sw = stg + r32 * 64 + 8 * hi + 16 * ((r32 >> 1) & 3);
#pragma unroll
      for (int i = 0; i < 12; ++i) *(LDS(u32x2)*)((sw ^ (16 * (i & 3))) + (i >> 2) * 2048) = w[i];
      asm volatile("s_waitcnt lgkmcnt(0)" ::: "memory");
      const int sr = lane >> 2, sk = lane & 3;
      const unsigned srd = stg + sr * 64 + 16 * (sk ^ ((sr >> 1) & 3));
      u32x4 z[6];
#pragma unroll
      for (int i = 0; i < 6; ++i) z[i] = *(const LDS(u32x4)*)(srd + i * 1024);
      SBAR();
      const unsigned go = (32 * wq + sr) * ROWB + 64 * half + 16 * sk;
#pragma unroll
      for (int i = 0; i < 6; ++i) __builtin_amdgcn_raw_buffer_store_b128(z[i], rsO, go + (i >> 1) * 128, sQ + (i & 1) * 16 * ROWB, STAUX);
    }
  } else if (active) {
    const unsigned stg = lds0 + (wid < 6 ? HALFB + wid * TILEB : (wid - 6) * SLOTB);
    const float inv = __builtin_amdgcn_rcpf(lsum);
    u32x2 w[24];
#pragma unroll
    for (int i = 0; i < 24; ++i) { const f32x16& z = o[i >> 2]; const int t = i & 3;
      w[i] = u32x2{cvtpk(z[4 * t] * inv, z[4 * t + 1] * inv), cvtpk(z[4 * t + 2] * inv, z[4 * t + 3] * inv)}; }
    SBAR();
    const unsigned sw = stg + r32 * 128 + 8 * hi + 16 * ((r32 & 7) ^ (4 * half));
#pragma unroll
    for (int i = 0; i < 24; ++i) *(LDS(u32x2)*)((sw ^ (16 * (i & 7))) + (i >> 3) * 4096) = w[i];
    asm volatile("s_waitcnt lgkmcnt(0)" ::: "memory");
    const unsigned srd = stg + rr * 128 + 16 * (dk ^ rr);
    u32x4 z[12];
#pragma unroll
    for (int i = 0; i < 12; ++i) z[i] = *(const LDS(u32x4)*)(srd + i * 1024);
    SBAR();
    const unsigned go = (32 * wid + rr) * ROWB + 16 * dk;
#pragma unroll
    for (int i = 0; i < 12; ++i) __builtin_amdgcn_raw_buffer_store_b128(z[i], rsO, go + (i >> 2) * 128, sQ + (i & 3) * 8 * ROWB, STAUX);
  }
  }
  STAMP(14);
  asm volatile("s_waitcnt vmcnt(0)" ::: "memory");
  STAMP(15);
  PROF_END();
}

__global__ void k0(unsigned long long* p) { if (threadIdx.x == 0 && blockIdx.x == 0) __hip_atomic_store(p, (unsigned long long)__builtin_amdgcn_s_memrealtime(), __ATOMIC_RELAXED, __HIP_MEMORY_SCOPE_AGENT); }
extern "C" void kernel_launch(void* const* d_in, const int* in_sizes, int n_in,
                              void* d_out, int out_size, void* d_ws, size_t ws_size,
                              hipStream_t stream) {
  const int T = in_sizes[0] / (NH * HD), nseq = in_sizes[3] - 1;
  (void)hipFuncSetAttribute((const void*)varlen_attn_kernel, hipFuncAttributeMaxDynamicSharedMemorySize, (int)LDS_BYTES);
  hipLaunchKernelGGL(k0, dim3(K0GRID), dim3(64), 0, stream, (unsigned long long*)d_ws);
  for (int s0 = 0; s0 < nseq; s0 += 8) {
    const int ns = nseq - s0 < 8 ? nseq - s0 : 8;
    int G = T / 256 + ns;
    if (G < 16) G = 16;
    hipLaunchKernelGGL(varlen_attn_kernel, dim3(NH * G), dim3(512), LDS_BYTES, stream,
                       (const __hip_bfloat16*)d_in[0], (const __hip_bfloat16*)d_in[1], (const __hip_bfloat16*)d_in[2],
                       (const int*)d_in[3] + s0, (__hip_bfloat16*)d_out, ns, G PROF_PASS);
  }
}
```

```cpp
#include <hip/hip_runtime.h>
#include <hip/hip_bf16.h>

typedef __attribute__((ext_vector_type(2))) int i32x2;
#define LDS(T) __attribute__((address_space(3))) T
#define DI __device__ __forceinline__

constexpr int NH = 16, HD = 192;
constexpr unsigned ROWB = NH * HD * 2;
constexpr unsigned HEADB = HD * 2;
constexpr unsigned TILEB = 32 * HEADB;
constexpr unsigned SLOTB = 2 * TILEB;
constexpr unsigned X_OFF = 6 * SLOTB;
constexpr unsigned SINK_OFF = X_OFF + TILEB;
constexpr unsigned LDS_BYTES = X_OFF + 16384;
constexpr unsigned TSTEP = 32 * ROWB;
constexpr unsigned OOB = 0xfff00000u;
#define STPOL "nt"
#define HELP_SLACK 3
#define RAMP 2
#define EARLY_KV 13

DI unsigned rfl(unsigned x) { return __builtin_amdgcn_readfirstlane(x); }
DI __amdgpu_buffer_rsrc_t mk_rsrc(const void* base, unsigned bytes) {
  return __builtin_amdgcn_make_buffer_rsrc((void*)base, (short)0, (int)bytes, 0x00020000);
}
#define DMA3P(POL, rs, voff, soff, ldsdst) \
  asm volatile("s_mov_b32 m0, %3\n\ts_nop 0\n\t" \
               "buffer_load_dwordx4 %0, %1, %2 offen " POL "lds\n\t" \
               "s_add_u32 m0, m0, 3968\n\ts_nop 0\n\t" \
               "buffer_load_dwordx4 %0, %1, %2 offen offset:128 " POL "lds\n\t" \
               "s_add_u32 m0, m0, 3968\n\ts_nop 0\n\t" \
               "buffer_load_dwordx4 %0, %1, %2 offen offset:256 " POL "lds" \
               :: "v"(voff), "s"(rs), "s"(soff), "s"(ldsdst) : "memory", "scc")
#define DMA3(rs, voff, soff, ldsdst) DMA3P("", rs, voff, soff, ldsdst)
#define DMA3Q(rs, voff, soff, ldsdst) DMA3P("nt ", rs, voff, soff, ldsdst)
#define DMA3S(GAP, rs, voff, soff, ldsdst, step) \
  asm volatile("s_mov_b32 m0, %3\n\t" GAP \
               "buffer_load_dwordx4 %0, %1, %2 offen lds\n\t" \
               "s_add_u32 m0, m0, %4\n\t" GAP \
               "buffer_load_dwordx4 %0, %1, %2 offen offset:128 lds\n\t" \
               "s_add_u32 m0, m0, %4\n\t" GAP \
               "buffer_load_dwordx4 %0, %1, %2 offen offset:256 lds" \
               :: "v"(voff), "s"(rs), "s"(soff), "s"(ldsdst), "s"(step) : "memory", "scc")
#define NULL3(rs, voff, ldsdst) \
  asm volatile("s_mov_b32 m0, %3\n\ts_nop 0\n\t" \
               "buffer_load_dwordx4 %0, %1, %2 offen lds\n\tbuffer_load_dwordx4 %0, %1, %2 offen lds\n\tbuffer_load_dwordx4 %0, %1, %2 offen lds" \
               :: "v"(voff), "s"(rs), "s"(OOB), "s"(ldsdst) : "memory")
#define WAIT_BAR(N) asm volatile("s_waitcnt vmcnt(" #N ")\n\ts_barrier" ::: "memory")
template <int CTRL> DI int dpp_add(int x) { return x + __builtin_amdgcn_update_dpp(0, x, CTRL, 0xf, 0xf, true); }

__global__ __launch_bounds__(512, 2) void varlen_attn_kernel(const __hip_bfloat16* __restrict__ Q, const __hip_bfloat16* __restrict__ K,
                                                            const __hip_bfloat16* __restrict__ V, const int* __restrict__ cu,
                                                            __hip_bfloat16* __restrict__ O, int nseq, int G, int T, unsigned rpw) {
  extern __shared__ __attribute__((aligned(1024))) char smem[];
  const unsigned lds0 = (unsigned)(size_t)(LDS(char)*)smem;
  const int lane = threadIdx.x & 63;
  const int wid = rfl(threadIdx.x >> 6), half = wid >> 2, wq = wid & 3;
  const int b = blockIdx.x;
  const unsigned sink = lds0 + SINK_OFF;
  const bool aide = b >= 64;
  if (aide && wid < 2) {
    unsigned long long pc; unsigned len;
    asm volatile("s_getpc_b64 %0\n.Lva_begin:\n\ts_mov_b32 %1, .Lva_end-.Lva_begin-4" : "=s"(pc), "=s"(len));
    asm volatile("s_mov_b32 m0, %2\n\ts_nop 0\n\tglobal_load_lds_dword %0, %1" :: "v"(min(threadIdx.x * 128u, len)), "s"(pc), "s"(sink) : "memory");
  }
  unsigned voffK, voffV, vq, vtouch, oa = (lane >> 3) * 4, oc = (lane & 7) * 4;
  { const int rr = lane >> 3, dk = lane & 7, drow = 8 * wq + rr;
    voffK = drow * ROWB + 16 * (dk ^ ((drow >> 1) & 7));
    voffV = drow * ROWB + 16 * (dk ^ (((drow >> 1) & 1) << 2));
    vq = rr * ROWB + 16 * (dk ^ (rr >> 1));
    vtouch = ((511 - threadIdx.x) >> 1) * ROWB + (lane & 1) * HEADB;
    asm volatile("" : "+v"(voffK), "+v"(voffV), "+v"(vq), "+v"(vtouch), "+v"(oa), "+v"(oc)); }
  asm volatile("" :: "s"(Q), "s"(K), "s"(V), "s"(cu), "s"(O), "s"(nseq), "s"(G), "s"(T), "s"(rpw));
  i32x2 ca, cc;
  { const __amdgpu_buffer_rsrc_t rsC = mk_rsrc(cu, 4 * nseq + 4);
    asm volatile("buffer_load_dwordx2 %0, %2, %4, 0 offen\n\tbuffer_load_dwordx2 %1, %3, %4, 0 offen" : "=&v"(ca), "=&v"(cc) : "v"(oa), "v"(oc), "s"(rsC) : "memory"); }
  const int tch = aide ? wid : 0;
  if (tch >= 5) {
    const unsigned r0 = (unsigned)((b >> 3) - 8) * rpw;
    const __amdgpu_buffer_rsrc_t rsT = mk_rsrc((const char*)Q + (b & 7) * 2 * HEADB, min(r0 + rpw, (unsigned)T) * ROWB - (b & 7) * 2 * HEADB);
    asm volatile("s_mov_b32 m0, %3\n\ts_nop 0\n\tbuffer_load_dword %0, %1, %2 offen lds" :: "v"(vtouch), "s"(rsT), "s"(r0 * ROWB), "s"(sink) : "memory");
  }
  const int head = 2 * (b & 7) + ((b >> 3) & 1), g = b >> 4;
  const char* kbase = (const char*)K + head * HEADB;
  const char* vbase = (const char*)V + head * HEADB;
  const char* qbase = (const char*)Q + head * HEADB;
  unsigned kb = lds0 + (lane & 31) * 128 + 16 * ((lane >> 5) ^ ((lane >> 1) & 7));
  unsigned vb = lds0 + TILEB + (4 * (lane >> 5) + ((lane >> 2) & 3)) * 128 + 64 * ((lane >> 3) & 1) + 32 * ((lane >> 4) & 1) + 8 * (lane & 3);
  unsigned sw = (lane & 31) * 128 + 8 * (((lane >> 5) ^ (lane >> 3)) & 1) + 16 * (lane & 7);
  unsigned srd = (lane >> 3) * 128 + 16 * ((lane ^ (lane >> 3)) & 7);
  unsigned go = (lane >> 3) * ROWB + 16 * (lane & 7);
  asm volatile("" : "+s"(kbase), "+s"(vbase), "+s"(qbase));
  const unsigned et = wid < 3 ? threadIdx.x : min(lane, EARLY_KV * 3 - 1);
  int e3 = (et * 43691u) >> 17; unsigned eoff = (et - 3 * e3) * 128;
  const char* ebase = wid < 3 ? qbase : wid == 3 ? kbase : vbase;
  asm volatile("" : "+s"(ebase));
  asm volatile("" : "+v"(kb), "+v"(vb), "+v"(sw), "+v"(srd), "+v"(go), "+v"(e3), "+v"(eoff));
  asm volatile("s_waitcnt vmcnt(1)\n\ts_cmp_ge_i32 %2, %3\n\ts_cbranch_scc1 .Lcw%=\n\ts_waitcnt vmcnt(0)\n.Lcw%=:" : "+v"(ca), "+v"(cc) : "s"(tch), "i"(5) : "memory", "scc");
  const int a0 = ca[0], la = max(ca[1] - ca[0], 0), lc = max(cc[1] - cc[0], 0);
  const int nb2a = (la + 255) >> 8, nbiga = ((la + 127) >> 7) - nb2a;
  const int nb2c = (lc + 255) >> 8, nbigc = ((lc + 127) >> 7) - nb2c;
  const int sma = ((la - 1) & 255) < 64, smc = ((lc - 1) & 255) < 64;
  int tot = nb2c + (nbigc << 12) + (smc << 24);
  int bef = (8 * lc - (lane & 7) > 8 * la - (lane >> 3)) ? tot : 0;
  bef = dpp_add<0xB1>(bef); tot = dpp_add<0xB1>(tot);
  bef = dpp_add<0x4E>(bef); tot = dpp_add<0x4E>(tot);
  bef = dpp_add<0x141>(bef); tot = dpp_add<0x141>(tot);
  const unsigned long long zf = __ballot(bef == 0 && la > 0);
  if (zf == 0) return;
  const int zi = __builtin_ctzll(zf), z0 = __builtin_amdgcn_readlane(a0, zi), zl = __builtin_amdgcn_readlane(la, zi);
  const int tots = rfl(tot), spare = G - (tots & 0xfff) + (tots >> 25);
  const int zc = 2 * min(spare, ((zl + 127) >> 7) - ((zl + 255) >> 8));
  int seq0 = z0, L = zl, qrow = g << 7, nq = min(128, zl - qrow);
  bool pair = false;
  if (g >= zc) {
    const int np = max(G - max(((tots >> 12) & 0xfff) - spare, 0) - zc, 1);
    if (wid < 5 && g - zc < np) {
      const __amdgpu_buffer_rsrc_t rsE = mk_rsrc(ebase, (unsigned)(z0 + zl - 1) * ROWB + HEADB);
      asm volatile("s_mov_b32 m0, %3\n\ts_nop 0\n\tbuffer_load_dword %0, %1, %2 offen lds"
                   :: "v"((unsigned)max(zl - 1 - (g - zc) - np * e3, 0) * ROWB + eoff), "s"(rsE), "s"((unsigned)z0 * ROWB), "s"(sink) : "memory");
    }
    const int smb = bef >> 24, bigbef = (bef >> 12) & 0xfff;
    const int nsplit = min(max(spare - bigbef, 0), nbiga);
    const int start = (bef & 0xfff) - (smb >> 1) + min(spare, bigbef);
    const int cnt = nb2a + nsplit - (sma & smb);
    const unsigned long long hit = __ballot(start <= g && g < start + cnt);
    if (hit == 0) return;
    int si = __builtin_ctzll(hit);
    int x = g - __builtin_amdgcn_readlane(start, si);
    if ((__ballot(sma && g == start + nb2a + nsplit - 1 && smb + 1 < (tots >> 24)) & hit)) {
      pair = true;
      const int nxt = __builtin_amdgcn_readlane(smb, si) + 1;
      if (wid & 2) { si = __builtin_ctzll(__ballot(sma && smb == nxt)); x = __builtin_amdgcn_readlane(cnt, si); }
    }
    const int ns = __builtin_amdgcn_readlane(nsplit, si);
    seq0 = __builtin_amdgcn_readlane(a0, si); L = __builtin_amdgcn_readlane(la, si);
    qrow = x < 2 * ns ? x << 7 : (x - ns) << 8;
    nq = min(x < 2 * ns ? 128 : 256, L - qrow);
    const int na = (la + 31) >> 5, nme = (L + 31) >> 5;
    int stp = nsplit < nbiga ? na : (na + 1) >> 1;
    stp = max(stp, __builtin_amdgcn_update_dpp(0, stp, 0x128, 0xf, 0xf, true));
    const int lim = max(max(__builtin_amdgcn_readlane(stp, 0), __builtin_amdgcn_readlane(stp, 16)), max(__builtin_amdgcn_readlane(stp, 32), __builtin_amdgcn_readlane(stp, 48))) - HELP_SLACK;
    if (!pair && si != zi && (nq > 128 ? nme : (nme + 1) >> 1) <= lim && g < 32) {
      const unsigned long long zs = __ballot(bigbef <= spare && spare < bigbef + nbiga);
      const unsigned all = (unsigned)(((1ull << cnt) - 1) << start), big = (unsigned)(((1ull << (nbiga - nsplit)) - 1) << (start + 2 * nsplit));
      const unsigned prm = (sma && !(smb & 1) && smb + 1 < (tots >> 24)) ? (unsigned)(1ull << (start + cnt - 1)) : 0u;
      const unsigned lo = all & ~big & ~prm;
      unsigned hm = bef == 0 ? 0u : (((na + 1) >> 1) <= lim ? lo : 0u) | (na <= lim ? big : 0u);
      unsigned h2 = bef == 0 ? 0u : (((na + 1) >> 1) < lim ? lo : 0u) | (na < lim ? big : 0u);
      hm |= __builtin_amdgcn_update_dpp(0, hm, 0x128, 0xf, 0xf, true); h2 |= __builtin_amdgcn_update_dpp(0, h2, 0x128, 0xf, 0xf, true);
      hm = __builtin_amdgcn_readlane(hm, 0) | __builtin_amdgcn_readlane(hm, 16) | __builtin_amdgcn_readlane(hm, 32) | __builtin_amdgcn_readlane(hm, 48);
      h2 = __builtin_amdgcn_readlane(h2, 0) | __builtin_amdgcn_readlane(h2, 16) | __builtin_amdgcn_readlane(h2, 32) | __builtin_amdgcn_readlane(h2, 48);
      const int hx = __builtin_popcount(hm & ((1u << g) - 1)) + __builtin_popcount(h2 & ((1u << g) - 1)), sh = ((h2 >> g) & 1);
      const int hu = __builtin_popcount(hm) + __builtin_popcount(h2);
      const unsigned t = threadIdx.x & 127, tr = (t * 43691u) >> 17;
      const int hidx = hx + (tr & sh) + hu * (tr >> sh) + 1;
      auto touch = [&](int y0, int yl, const char* tb, int skip) {
        const __amdgpu_buffer_rsrc_t rsZ = mk_rsrc(tb, (unsigned)(y0 + yl - 1) * ROWB + HEADB);
        for (int top = yl - skip, i = 0; top > hx + (wid & 1) * hu * (21 >> sh) && i < 4; top -= hu * (42 >> sh), ++i)
          asm volatile("s_mov_b32 m0, %3\n\ts_nop 0\n\tbuffer_load_dword %0, %1, %2 offen lds"
                       :: "v"((unsigned)max(top - hidx, 0) * ROWB + (t - 3 * tr) * 128), "s"(rsZ), "s"((unsigned)y0 * ROWB), "s"(sink) : "memory");
      };
      const char* tb = wid < 4 ? qbase : wid < 6 ? kbase : vbase;
      if (wid >= 4) touch(z0, zl, tb, np * EARLY_KV);
      if (zs != 0 && (wid < 2 || (wid >= 4 && (zs & __ballot(na >= lim + HELP_SLACK)) != 0)))
        touch(__builtin_amdgcn_readlane(a0, __builtin_ctzll(zs)), __builtin_amdgcn_readlane(la, __builtin_ctzll(zs)), tb, 0);
    }
  }
  const bool duo = nq > 128;
  const int n = (L + 31) >> 5;
  const unsigned kvbytes = (unsigned)(seq0 + L - 1) * ROWB + HEADB;
  const __amdgpu_buffer_rsrc_t rsK = mk_rsrc(kbase, kvbytes), rsV = mk_rsrc(vbase, kvbytes);
  const unsigned s0 = (unsigned)(seq0 + 32 * (n - 1)) * ROWB;
  const unsigned sQ = (unsigned)(seq0 + qrow) * ROWB, qbytes = sQ + (unsigned)(nq - 1) * ROWB + HEADB;
  const __amdgpu_buffer_rsrc_t rsQ = mk_rsrc(qbase, qbytes);
  const unsigned rbase = pair ? (wid & 2) * (3 * SLOTB / 2) : 0;
  const unsigned dK = lds0 + rbase + wq * 1024, dV = dK + TILEB;
  const int qt = duo ? wid : pair ? wid & 1 : wq;
  const unsigned qo = pair ? 2 * SLOTB + qt * TILEB : X_OFF - qt * TILEB, sq = sQ + 32 * qt * ROWB;
  const unsigned o16 = (wq & 2) ? -16 * ROWB : 16 * ROWB;
  const bool rows = (duo || half == 0) && 32 * qt < nq;
  if (!duo && half == 0 && !rows) return;
  if (duo != (half != 0)) DMA3(rsK, voffK, s0, dK);
  if (pair && half) DMA3(rsK, voffK + o16, s0, dK ^ 2048);
  if (duo || half == 0) { DMA3Q(rsQ, vq, sq, lds0 + rbase + qo); DMA3Q(rsQ, vq ^ 64, sq + 8 * ROWB, lds0 + rbase + qo + 1024); DMA3Q(rsQ, vq, sq + 16 * ROWB, lds0 + rbase + qo + 2048); }
  if (duo || half) DMA3Q(rsQ, vq ^ 64, sq + 24 * ROWB, lds0 + rbase + qo + 3072);
  asm volatile("s_barrier" ::: "memory");
  const __amdgpu_buffer_rsrc_t rsD = half ? rsV : rsK;
  const unsigned voffD = half ? voffV : voffK, dD = half ? dV : dK;
  if (duo) DMA3(rsD, voffD, s0 - (1 - half) * TSTEP, dD + (1 - half) * SLOTB);
  if (rows) {
    const unsigned stg = lds0 + rbase + (qt < 6 ? qt * SLOTB : (unsigned)((n + qt - 5) % 6) * SLOTB + TILEB);
    const __amdgpu_buffer_rsrc_t rsO = mk_rsrc((char*)O + head * HEADB, qbytes);
    const unsigned bias = (lane & 31) >= L - 32 * (n - 1) ? 0xff80u : 0u;
    asm volatile(
    "v_mov_b32 v232, 0\n"
    "v_mov_b32 v233, 0\n"
    "v_mov_b32 v234, 0\n"
    "v_mov_b32 v235, 0\n"
    "v_mov_b32 v216, 0x3f803f80\n"
    "v_mov_b32 v217, 0x3f803f80\n"
    "v_mov_b32 v218, 0x3f803f80\n"
    "v_mov_b32 v219, 0x3f803f80\n"
    "v_mov_b32 v220, %[bias]\n"
    "v_mov_b32 v221, 0\n"
    "v_mov_b32 v222, 0\n"
    "v_mov_b32 v223, 0\n"
    "v_mov_b32 v237, 0\n"
    "s_mov_b32 s64, 0x3dd53b94\n"
    "v_mfma_f32_32x32x16_bf16 v[96:111], v[220:223], v[216:219], 0\n"
    "v_mfma_f32_32x32x16_bf16 v[0:15], v[232:235], v[232:235], 0\n"
    "v_mfma_f32_32x32x16_bf16 v[16:31], v[232:235], v[232:235], 0\n"
    "v_mfma_f32_32x32x16_bf16 v[32:47], v[232:235], v[232:235], 0\n"
    "v_mfma_f32_32x32x16_bf16 v[48:63], v[232:235], v[232:235], 0\n"
    "v_mfma_f32_32x32x16_bf16 v[64:79], v[232:235], v[232:235], 0\n"
    "v_mfma_f32_32x32x16_bf16 v[80:95], v[232:235], v[232:235], 0\n"
    "v_add_u32 v208, %[qlds], %[kb]\n"
    "v_mov_b32 v240, %[kb]\n"
    "v_xor_b32 v209, 32, v208\n"
    "v_xor_b32 v241, 32, %[kb]\n"
    "v_xor_b32 v210, 64, v208\n"
    "v_xor_b32 v242, 64, %[kb]\n"
    "v_xor_b32 v211, 0x60, v208\n"
    "v_xor_b32 v243, 0x60, %[kb]\n"
    "v_mov_b32 v244, %[vb]\n"
    "v_xor_b32 v245, 64, %[vb]\n"
    "s_mov_b32 s65, 0x44800000\n"
    "s_mov_b32 s66, 24576\n"
    "s_add_i32 s68, %[n], -1\n"
    "s_mov_b32 s70, %[duo]\n"
    "s_mov_b32 s71, %[sa]\n"
    "s_mov_b32 s72, %[dd]\n"
    "s_mov_b32 s73, %[rem]\n"
    "s_waitcnt vmcnt(3)\n"
    "s_cmp_lg_u32 %[duo], 0\n"
    "s_cbranch_scc1 .Lwa%=\n"
    "s_waitcnt vmcnt(0)\n"
    ".Lwa%=:\n"
    "s_barrier\n"
    "ds_read_b128 v[112:115], v208 offset:0\n"
    "ds_read_b128 v[160:163], v240 offset:0\n"
    "ds_read_b128 v[116:119], v209 offset:0\n"
    "ds_read_b128 v[164:167], v241 offset:0\n"
    "ds_read_b128 v[120:123], v210 offset:0\n"
    "ds_read_b128 v[168:171], v242 offset:0\n"
    "ds_read_b128 v[124:127], v211 offset:0\n"
    "ds_read_b128 v[172:175], v243 offset:0\n"
    "ds_read_b128 v[128:131], v208 offset:4096\n"
    "ds_read_b128 v[176:179], v240 offset:4096\n"
    "ds_read_b128 v[132:135], v209 offset:4096\n"
    "ds_read_b128 v[180:183], v241 offset:4096\n"
    "s_waitcnt lgkmcnt(10)\n"
    "v_mfma_f32_32x32x16_bf16 v[96:111], v[160:163], v[112:115], v[96:111]\n"
    "ds_read_b128 v[136:139], v210 offset:4096\n"
    "ds_read_b128 v[184:187], v242 offset:4096\n"
    "s_waitcnt lgkmcnt(10)\n"
    "v_mfma_f32_32x32x16_bf16 v[96:111], v[164:167], v[116:119], v[96:111]\n"
    "ds_read_b128 v[140:143], v211 offset:4096\n"
    "ds_read_b128 v[188:191], v243 offset:4096\n"
    "s_waitcnt lgkmcnt(10)\n"
    "v_mfma_f32_32x32x16_bf16 v[96:111], v[168:171], v[120:123], v[96:111]\n"
    "ds_read_b128 v[144:147], v208 offset:8192\n"
    "ds_read_b128 v[192:195], v240 offset:8192\n"
    "s_waitcnt lgkmcnt(10)\n"
    "v_mfma_f32_32x32x16_bf16 v[96:111], v[172:175], v[124:127], v[96:111]\n"
    "ds_read_b128 v[148:151], v209 offset:8192\n"
    "ds_read_b128 v[196:199], v241 offset:8192\n"
    "s_waitcnt lgkmcnt(10)\n"
    "v_mfma_f32_32x32x16_bf16 v[96:111], v[176:179], v[128:131], v[96:111]\n"
    "ds_read_b128 v[152:155], v210 offset:8192\n"
    "ds_read_b128 v[200:203], v242 offset:8192\n"
    "s_waitcnt lgkmcnt(10)\n"
    "v_mfma_f32_32x32x16_bf16 v[96:111], v[180:183], v[132:135], v[96:111]\n"
    "ds_read_b128 v[156:159], v211 offset:8192\n"
    "ds_read_b128 v[204:207], v243 offset:8192\n"
    "s_waitcnt lgkmcnt(10)\n"
    "v_mfma_f32_32x32x16_bf16 v[96:111], v[184:187], v[136:139], v[96:111]\n"
    "v_add_u32 v240, 24576, %[kb]\n"
    "v_xor_b32 v241, 32, v240\n"
    "v_xor_b32 v242, 64, v240\n"
    "v_xor_b32 v243, 0x60, v240\n"
    "s_waitcnt lgkmcnt(8)\n"
    "v_mfma_f32_32x32x16_bf16 v[96:111], v[188:191], v[140:143], v[96:111]\n"
    "s_waitcnt vmcnt(0) lgkmcnt(0)\n"
    "s_barrier\n"
    "v_mfma_f32_32x32x16_bf16 v[96:111], v[192:195], v[144:147], v[96:111]\n"
    "ds_read_b128 v[160:163], v240 offset:0\n"
    "ds_read_b128 v[164:167], v241 offset:0\n"
    "v_mfma_f32_32x32x16_bf16 v[96:111], v[196:199], v[148:151], v[96:111]\n"
    "ds_read_b128 v[168:171], v242 offset:0\n"
    "ds_read_b128 v[172:175], v243 offset:0\n"
    "v_mfma_f32_32x32x16_bf16 v[96:111], v[200:203], v[152:155], v[96:111]\n"
    "v_mfma_f32_32x32x16_bf16 v[96:111], v[204:207], v[156:159], v[96:111]\n"
    "s_cmp_eq_u32 %[duo], 0\n"
    "s_cbranch_scc1 .Lnd%=\n"
    "s_cmp_gt_i32 s73, 0\n"
    "s_cselect_b32 s74, s71, %[oob]\n"
    "s_cselect_b32 s75, s72, %[dump]\n"
    "s_add_u32 m0, s75, 0\n"
    "s_nop 0\n"
    "buffer_load_dwordx4 %[voff], %[rkv], s74 offen offset:0 lds\n"
    "s_add_u32 m0, s75, 3968\n"
    "s_nop 0\n"
    "buffer_load_dwordx4 %[voff], %[rkv], s74 offen offset:128 lds\n"
    "s_add_u32 m0, s75, 7936\n"
    "s_nop 0\n"
    "buffer_load_dwordx4 %[voff], %[rkv], s74 offen offset:256 lds\n"
    "s_add_i32 s73, s73, -1\n"
    "s_add_u32 s71, s71, 0xfffd0000\n"
    "s_add_u32 s72, s72, 24576\n"
    "s_cmp_lt_u32 s72, %[dend]\n"
    "s_cselect_b32 s72, s72, %[dd0]\n"
    ".Lnd%=:\n"
    "s_nop 11\n"
    "v_max3_f32 v232, v96, v97, v98\n"
    "v_max3_f32 v233, v99, v100, v101\n"
    "v_max3_f32 v234, v102, v103, v104\n"
    "v_max3_f32 v235, v105, v106, v107\n"
    "v_max3_f32 v238, v108, v109, v110\n"
    "v_max3_f32 v232, v232, v233, v234\n"
    "v_max3_f32 v238, v238, v111, v235\n"
    "v_max_f32 v238, v238, v232\n"
    "v_mov_b32 v232, v238\n"
    "s_nop 1\n"
    "v_permlane32_swap_b32 v238, v232\n"
    "s_nop 1\n"
    "v_max_f32 v238, v238, v232\n"
    "v_mul_f32 v238, s64, v238\n"
    "v_fma_f32 v216, v96, s64, -v238\n"
    "v_fma_f32 v217, v97, s64, -v238\n"
    "v_fma_f32 v218, v98, s64, -v238\n"
    "v_fma_f32 v219, v99, s64, -v238\n"
    "v_fma_f32 v220, v100, s64, -v238\n"
    "v_fma_f32 v221, v101, s64, -v238\n"
    "v_fma_f32 v222, v102, s64, -v238\n"
    "v_fma_f32 v223, v103, s64, -v238\n"
    "v_fma_f32 v224, v104, s64, -v238\n"
    "v_fma_f32 v225, v105, s64, -v238\n"
    "v_fma_f32 v226, v106, s64, -v238\n"
    "v_fma_f32 v227, v107, s64, -v238\n"
    "v_fma_f32 v228, v108, s64, -v238\n"
    "v_fma_f32 v229, v109, s64, -v238\n"
    "v_fma_f32 v230, v110, s64, -v238\n"
    "v_fma_f32 v231, v111, s64, -v238\n"
    "v_exp_f32 v232, v216\n"
    "v_exp_f32 v233, v217\n"
    "v_exp_f32 v234, v218\n"
    "v_add_f32 v236, v232, v233\n"
    "v_cvt_pk_bf16_f32 v208, v232, v233\n"
    "v_exp_f32 v235, v219\n"
    "v_add_f32 v236, v236, v234\n"
    "v_exp_f32 v232, v220\n"
    "v_add_f32 v236, v236, v235\n"
    "v_cvt_pk_bf16_f32 v209, v234, v235\n"
    "v_exp_f32 v233, v221\n"
    "v_add_f32 v236, v236, v232\n"
    "v_exp_f32 v234, v222\n"
    "v_add_f32 v236, v236, v233\n"
    "v_cvt_pk_bf16_f32 v210, v232, v233\n"
    "v_exp_f32 v235, v223\n"
    "v_add_f32 v236, v236, v234\n"
    "s_waitcnt lgkmcnt(0)\n"
    "s_cmp_eq_u32 s68, 0\n"
    "s_cbranch_scc1 .Ltail%=\n"
    "s_cmp_lg_u32 %[duo], 0\n"
    "s_cbranch_scc1 .Lloopd%=\n"
    ".p2align 6, , 60\n"
    ".Lloopl%=:\n"
    "v_mfma_f32_32x32x16_bf16 v[96:111], v[160:163], v[112:115], 0\n"
    "ds_read_b128 v[160:163], v240 offset:4096\n"
    "ds_read_b64_tr_b16 v[176:177], v244 offset:0\n"
    "v_exp_f32 v232, v224\n"
    "v_add_f32 v236, v236, v235\n"
    "v_cvt_pk_bf16_f32 v211, v234, v235\n"
    "v_mfma_f32_32x32x16_bf16 v[96:111], v[164:167], v[116:119], v[96:111]\n"
    "ds_read_b128 v[164:167], v241 offset:4096\n"
    "ds_read_b64_tr_b16 v[178:179], v244 offset:1024\n"
    "v_exp_f32 v233, v225\n"
    "v_add_f32 v236, v236, v232\n"
    "s_waitcnt lgkmcnt(4)\n"
    "v_mfma_f32_32x32x16_bf16 v[96:111], v[168:171], v[120:123], v[96:111]\n"
    "ds_read_b128 v[168:171], v242 offset:4096\n"
    "ds_read_b64_tr_b16 v[180:181], v245 offset:0\n"
    "v_exp_f32 v234, v226\n"
    "v_add_f32 v236, v236, v233\n"
    "v_cvt_pk_bf16_f32 v212, v232, v233\n"
    "v_mfma_f32_32x32x16_bf16 v[96:111], v[172:175], v[124:127], v[96:111]\n"
    "ds_read_b128 v[172:175], v243 offset:4096\n"
    "ds_read_b64_tr_b16 v[182:183], v245 offset:1024\n"
    "v_exp_f32 v235, v227\n"
    "v_add_f32 v236, v236, v234\n"
    "s_waitcnt lgkmcnt(5)\n"
    "v_mfma_f32_32x32x16_bf16 v[96:111], v[160:163], v[128:131], v[96:111]\n"
    "ds_read_b128 v[160:163], v240 offset:8192\n"
    "ds_read_b64_tr_b16 v[184:185], v244 offset:4096\n"
    "v_exp_f32 v232, v228\n"
    "v_add_f32 v236, v236, v235\n"
    "v_cvt_pk_bf16_f32 v213, v234, v235\n"
    "v_mfma_f32_32x32x16_bf16 v[96:111], v[164:167], v[132:135], v[96:111]\n"
    "ds_read_b128 v[164:167], v241 offset:8192\n"
    "ds_read_b64_tr_b16 v[186:187], v244 offset:5120\n"
    "v_exp_f32 v233, v229\n"
    "v_add_f32 v236, v236, v232\n"
    "s_waitcnt lgkmcnt(5)\n"
    "v_mfma_f32_32x32x16_bf16 v[96:111], v[168:171], v[136:139], v[96:111]\n"
    "ds_read_b128 v[168:171], v242 offset:8192\n"
    "ds_read_b64_tr_b16 v[188:189], v245 offset:4096\n"
    "v_exp_f32 v234, v230\n"
    "v_add_f32 v236, v236, v233\n"
    "v_cvt_pk_bf16_f32 v214, v232, v233\n"
    "v_mfma_f32_32x32x16_bf16 v[96:111], v[172:175], v[140:143], v[96:111]\n"
    "ds_read_b128 v[172:175], v243 offset:8192\n"
    "ds_read_b64_tr_b16 v[190:191], v245 offset:5120\n"
    "v_exp_f32 v235, v231\n"
    "v_add_f32 v236, v236, v234\n"
    "s_waitcnt lgkmcnt(5)\n"
    "v_mfma_f32_32x32x16_bf16 v[96:111], v[160:163], v[144:147], v[96:111]\n"
    "ds_read_b64_tr_b16 v[192:193], v244 offset:8192\n"
    "ds_read_b64_tr_b16 v[194:195], v244 offset:9216\n"
    "v_add_f32 v236, v236, v235\n"
    "v_cvt_pk_bf16_f32 v215, v234, v235\n"
    "v_mfma_f32_32x32x16_bf16 v[96:111], v[164:167], v[148:151], v[96:111]\n"
    "ds_read_b64_tr_b16 v[196:197], v245 offset:8192\n"
    "ds_read_b64_tr_b16 v[198:199], v245 offset:9216\n"
    "v_cmp_nge_f32 vcc, s65, v236\n"
    "s_waitcnt lgkmcnt(4)\n"
    "v_mfma_f32_32x32x16_bf16 v[96:111], v[168:171], v[152:155], v[96:111]\n"
    "ds_read_b64_tr_b16 v[200:201], v244 offset:2048\n"
    "ds_read_b64_tr_b16 v[202:203], v244 offset:3072\n"
    "s_cbranch_vccnz .Lrare%=\n"
    ".Lbackl%=:\n"
    "v_add_f32 v237, v237, v236\n"
    "v_mfma_f32_32x32x16_bf16 v[96:111], v[172:175], v[156:159], v[96:111]\n"
    "ds_read_b64_tr_b16 v[204:205], v245 offset:2048\n"
    "ds_read_b64_tr_b16 v[206:207], v245 offset:3072\n"
    "s_add_i32 s67, s66, 24576\n"
    "s_cmp_lg_u32 s66, %[wrap]\n"
    "s_cselect_b32 s67, s67, 0\n"
    "v_mfma_f32_32x32x16_bf16 v[0:15], v[176:179], v[208:211], v[0:15]\n"
    "ds_read_b64_tr_b16 v[176:177], v244 offset:6144\n"
    "ds_read_b64_tr_b16 v[178:179], v244 offset:7168\n"
    "v_add_u32 v240, s67, %[kb]\n"
    "v_xor_b32 v241, 32, v240\n"
    "s_waitcnt lgkmcnt(6)\n"
    "v_mfma_f32_32x32x16_bf16 v[16:31], v[180:183], v[208:211], v[16:31]\n"
    "ds_read_b64_tr_b16 v[180:181], v245 offset:6144\n"
    "ds_read_b64_tr_b16 v[182:183], v245 offset:7168\n"
    "v_xor_b32 v242, 64, v240\n"
    "v_xor_b32 v243, 0x60, v240\n"
    "s_add_i32 s68, s68, -1\n"
    "v_mfma_f32_32x32x16_bf16 v[32:47], v[184:187], v[208:211], v[32:47]\n"
    "ds_read_b64_tr_b16 v[184:185], v244 offset:10240\n"
    "ds_read_b64_tr_b16 v[186:187], v244 offset:11264\n"
    "v_fma_f32 v216, v96, s64, -v238\n"
    "v_fma_f32 v217, v97, s64, -v238\n"
    "v_fma_f32 v218, v98, s64, -v238\n"
    "v_mfma_f32_32x32x16_bf16 v[48:63], v[188:191], v[208:211], v[48:63]\n"
    "ds_read_b64_tr_b16 v[188:189], v245 offset:10240\n"
    "ds_read_b64_tr_b16 v[190:191], v245 offset:11264\n"
    "v_fma_f32 v219, v99, s64, -v238\n"
    "v_fma_f32 v220, v100, s64, -v238\n"
    "s_waitcnt lgkmcnt(8)\n"
    "v_mfma_f32_32x32x16_bf16 v[64:79], v[192:195], v[208:211], v[64:79]\n"
    "v_exp_f32 v232, v216\n"
    "v_fma_f32 v221, v101, s64, -v238\n"
    "v_fma_f32 v222, v102, s64, -v238\n"
    "v_add_u32 v244, s66, %[vb]\n"
    "s_mov_b32 s66, s67\n"
    "v_mfma_f32_32x32x16_bf16 v[80:95], v[196:199], v[208:211], v[80:95]\n"
    "v_exp_f32 v233, v217\n"
    "v_fma_f32 v223, v103, s64, -v238\n"
    "v_fma_f32 v224, v104, s64, -v238\n"
    "v_fma_f32 v225, v105, s64, -v238\n"
    "v_xor_b32 v245, 64, v244\n"
    "v_mfma_f32_32x32x16_bf16 v[0:15], v[200:203], v[212:215], v[0:15]\n"
    "s_waitcnt lgkmcnt(0)\n"
    "s_barrier\n"
    "v_exp_f32 v234, v218\n"
    "v_add_f32 v236, v232, v233\n"
    "v_cvt_pk_bf16_f32 v208, v232, v233\n"
    "v_mfma_f32_32x32x16_bf16 v[16:31], v[204:207], v[212:215], v[16:31]\n"
    "ds_read_b128 v[160:163], v240 offset:0\n"
    "v_exp_f32 v235, v219\n"
    "v_fma_f32 v226, v106, s64, -v238\n"
    "v_fma_f32 v227, v107, s64, -v238\n"
    "v_add_f32 v236, v236, v234\n"
    "v_mfma_f32_32x32x16_bf16 v[32:47], v[176:179], v[212:215], v[32:47]\n"
    "ds_read_b128 v[164:167], v241 offset:0\n"
    "v_exp_f32 v232, v220\n"
    "v_fma_f32 v228, v108, s64, -v238\n"
    "v_add_f32 v236, v236, v235\n"
    "v_cvt_pk_bf16_f32 v209, v234, v235\n"
    "v_mfma_f32_32x32x16_bf16 v[48:63], v[180:183], v[212:215], v[48:63]\n"
    "ds_read_b128 v[168:171], v242 offset:0\n"
    "ds_read_b128 v[172:175], v243 offset:0\n"
    "v_exp_f32 v233, v221\n"
    "v_fma_f32 v229, v109, s64, -v238\n"
    "v_add_f32 v236, v236, v232\n"
    "v_mfma_f32_32x32x16_bf16 v[64:79], v[184:187], v[212:215], v[64:79]\n"
    "v_exp_f32 v234, v222\n"
    "v_fma_f32 v230, v110, s64, -v238\n"
    "v_add_f32 v236, v236, v233\n"
    "v_cvt_pk_bf16_f32 v210, v232, v233\n"
    "s_cmp_lg_u32 s68, 0\n"
    "v_mfma_f32_32x32x16_bf16 v[80:95], v[188:191], v[212:215], v[80:95]\n"
    "v_exp_f32 v235, v223\n"
    "v_fma_f32 v231, v111, s64, -v238\n"
    "v_add_f32 v236, v236, v234\n"
    "s_waitcnt lgkmcnt(2)\n"
    "s_cbranch_scc1 .Lloopl%=\n"
    ".Ltail%=:\n"
    "ds_read_b64_tr_b16 v[112:113], v244 offset:0\n"
    "ds_read_b64_tr_b16 v[114:115], v244 offset:1024\n"
    "ds_read_b64_tr_b16 v[116:117], v244 offset:2048\n"
    "ds_read_b64_tr_b16 v[118:119], v244 offset:3072\n"
    "ds_read_b64_tr_b16 v[120:121], v245 offset:0\n"
    "ds_read_b64_tr_b16 v[122:123], v245 offset:1024\n"
    "ds_read_b64_tr_b16 v[124:125], v245 offset:2048\n"
    "ds_read_b64_tr_b16 v[126:127], v245 offset:3072\n"
    "v_exp_f32 v232, v224\n"
    "v_add_f32 v236, v236, v235\n"
    "v_cvt_pk_bf16_f32 v211, v234, v235\n"
    "v_exp_f32 v233, v225\n"
    "v_add_f32 v236, v236, v232\n"
    "v_exp_f32 v234, v226\n"
    "v_add_f32 v236, v236, v233\n"
    "v_cvt_pk_bf16_f32 v212, v232, v233\n"
    "v_exp_f32 v235, v227\n"
    "v_add_f32 v236, v236, v234\n"
    "ds_read_b64_tr_b16 v[128:129], v244 offset:4096\n"
    "ds_read_b64_tr_b16 v[130:131], v244 offset:5120\n"
    "ds_read_b64_tr_b16 v[132:133], v244 offset:6144\n"
    "ds_read_b64_tr_b16 v[134:135], v244 offset:7168\n"
    "ds_read_b64_tr_b16 v[136:137], v245 offset:4096\n"
    "s_waitcnt lgkmcnt(11)\n"
    "ds_read_b64_tr_b16 v[138:139], v245 offset:5120\n"
    "v_exp_f32 v232, v228\n"
    "v_add_f32 v236, v236, v235\n"
    "v_cvt_pk_bf16_f32 v213, v234, v235\n"
    "v_exp_f32 v233, v229\n"
    "v_add_f32 v236, v236, v232\n"
    "v_exp_f32 v234, v230\n"
    "v_add_f32 v236, v236, v233\n"
    "v_cvt_pk_bf16_f32 v214, v232, v233\n"
    "v_exp_f32 v235, v231\n"
    "v_add_f32 v236, v236, v234\n"
    "s_mov_b32 s70, 2\n"
    "v_add_f32 v236, v236, v235\n"
    "v_cvt_pk_bf16_f32 v215, v234, v235\n"
    "v_cmp_nge_f32 vcc, s65, v236\n"
    "s_cbranch_vccnz .Lrare%=\n"
    ".Lbackt%=:\n"
    "v_add_f32 v237, v237, v236\n"
    "v_mfma_f32_32x32x16_bf16 v[0:15], v[112:115], v[208:211], v[0:15]\n"
    "ds_read_b64_tr_b16 v[140:141], v245 offset:6144\n"
    "ds_read_b64_tr_b16 v[142:143], v245 offset:7168\n"
    "ds_read_b64_tr_b16 v[144:145], v244 offset:8192\n"
    "s_waitcnt lgkmcnt(11)\n"
    "ds_read_b64_tr_b16 v[146:147], v244 offset:9216\n"
    "v_mov_b32 v232, v237\n"
    "v_mov_b32 v200, %[sw]\n"
    "v_xor_b32 v201, 16, %[sw]\n"
    "v_mfma_f32_32x32x16_bf16 v[0:15], v[116:119], v[212:215], v[0:15]\n"
    "ds_read_b64_tr_b16 v[148:149], v244 offset:10240\n"
    "ds_read_b64_tr_b16 v[150:151], v244 offset:11264\n"
    "ds_read_b64_tr_b16 v[152:153], v245 offset:8192\n"
    "s_waitcnt lgkmcnt(11)\n"
    "ds_read_b64_tr_b16 v[154:155], v245 offset:9216\n"
    "v_permlane32_swap_b32 v237, v232\n"
    "v_xor_b32 v202, 32, %[sw]\n"
    "v_xor_b32 v203, 48, %[sw]\n"
    "v_mfma_f32_32x32x16_bf16 v[16:31], v[120:123], v[208:211], v[16:31]\n"
    "ds_read_b64_tr_b16 v[156:157], v245 offset:10240\n"
    "ds_read_b64_tr_b16 v[158:159], v245 offset:11264\n"
    "v_add_f32 v237, v237, v232\n"
    "v_xor_b32 v204, 64, %[sw]\n"
    "v_rcp_f32 v234, v237\n"
    "v_xor_b32 v205, 0x50, %[sw]\n"
    "v_mfma_f32_32x32x16_bf16 v[16:31], v[124:127], v[212:215], v[16:31]\n"
    "v_xor_b32 v206, 0x60, %[sw]\n"
    "v_xor_b32 v207, 0x70, %[sw]\n"
    "s_mov_b32 s74, %[so]\n"
    "s_add_u32 s75, %[so], 49152\n"
    "s_add_u32 s71, %[so], 98304\n"
    "s_add_u32 s72, %[so], 147456\n"
    "v_mfma_f32_32x32x16_bf16 v[32:47], v[128:131], v[208:211], v[32:47]\n"
    "v_add_u32 v216, 0, %[srd]\n"
    "v_add_u32 v217, 2048, %[srd]\n"
    "v_add_u32 v218, 4096, %[srd]\n"
    "v_add_u32 v219, 6144, %[srd]\n"
    "v_add_u32 v220, 8192, %[srd]\n"
    "v_add_u32 v221, 10240, %[srd]\n"
    "s_waitcnt lgkmcnt(12)\n"
    "v_mfma_f32_32x32x16_bf16 v[32:47], v[132:135], v[212:215], v[32:47]\n"
    "v_pk_mul_f32 v[0:1], v[0:1], v[234:235] op_sel_hi:[1,0]\n"
    "v_pk_mul_f32 v[2:3], v[2:3], v[234:235] op_sel_hi:[1,0]\n"
    "s_waitcnt lgkmcnt(10)\n"
    "v_mfma_f32_32x32x16_bf16 v[48:63], v[136:139], v[208:211], v[48:63]\n"
    "v_pk_mul_f32 v[4:5], v[4:5], v[234:235] op_sel_hi:[1,0]\n"
    "v_pk_mul_f32 v[6:7], v[6:7], v[234:235] op_sel_hi:[1,0]\n"
    "v_cvt_pk_bf16_f32 v96, v0, v1\n"
    "v_cvt_pk_bf16_f32 v97, v2, v3\n"
    "s_waitcnt lgkmcnt(8)\n"
    "v_mfma_f32_32x32x16_bf16 v[48:63], v[140:143], v[212:215], v[48:63]\n"
    "ds_write_b64 v200, v[96:97] offset:0\n"
    "v_pk_mul_f32 v[8:9], v[8:9], v[234:235] op_sel_hi:[1,0]\n"
    "v_pk_mul_f32 v[10:11], v[10:11], v[234:235] op_sel_hi:[1,0]\n"
    "v_cvt_pk_bf16_f32 v98, v4, v5\n"
    "v_cvt_pk_bf16_f32 v99, v6, v7\n"
    "s_waitcnt lgkmcnt(7)\n"
    "v_mfma_f32_32x32x16_bf16 v[64:79], v[144:147], v[208:211], v[64:79]\n"
    "ds_write_b64 v201, v[98:99] offset:0\n"
    "v_pk_mul_f32 v[12:13], v[12:13], v[234:235] op_sel_hi:[1,0]\n"
    "v_pk_mul_f32 v[14:15], v[14:15], v[234:235] op_sel_hi:[1,0]\n"
    "v_cvt_pk_bf16_f32 v100, v8, v9\n"
    "v_cvt_pk_bf16_f32 v101, v10, v11\n"
    "s_waitcnt lgkmcnt(6)\n"
    "v_mfma_f32_32x32x16_bf16 v[64:79], v[148:151], v[212:215], v[64:79]\n"
    "ds_write_b64 v202, v[100:101] offset:0\n"
    "v_pk_mul_f32 v[16:17], v[16:17], v[234:235] op_sel_hi:[1,0]\n"
    "v_pk_mul_f32 v[18:19], v[18:19], v[234:235] op_sel_hi:[1,0]\n"
    "v_cvt_pk_bf16_f32 v102, v12, v13\n"
    "v_cvt_pk_bf16_f32 v103, v14, v15\n"
    "s_waitcnt lgkmcnt(5)\n"
    "v_mfma_f32_32x32x16_bf16 v[80:95], v[152:155], v[208:211], v[80:95]\n"
    "ds_write_b64 v203, v[102:103] offset:0\n"
    "v_pk_mul_f32 v[20:21], v[20:21], v[234:235] op_sel_hi:[1,0]\n"
    "v_pk_mul_f32 v[22:23], v[22:23], v[234:235] op_sel_hi:[1,0]\n"
    "v_cvt_pk_bf16_f32 v104, v16, v17\n"
    "v_cvt_pk_bf16_f32 v105, v18, v19\n"
    "s_waitcnt lgkmcnt(4)\n"
    "v_mfma_f32_32x32x16_bf16 v[80:95], v[156:159], v[212:215], v[80:95]\n"
    "ds_write_b64 v204, v[104:105] offset:0\n"
    "v_pk_mul_f32 v[24:25], v[24:25], v[234:235] op_sel_hi:[1,0]\n"
    "v_pk_mul_f32 v[26:27], v[26:27], v[234:235] op_sel_hi:[1,0]\n"
    "v_cvt_pk_bf16_f32 v106, v20, v21\n"
    "v_cvt_pk_bf16_f32 v107, v22, v23\n"
    "ds_write_b64 v205, v[106:107] offset:0\n"
    "v_pk_mul_f32 v[28:29], v[28:29], v[234:235] op_sel_hi:[1,0]\n"
    "v_pk_mul_f32 v[30:31], v[30:31], v[234:235] op_sel_hi:[1,0]\n"
    "v_cvt_pk_bf16_f32 v108, v24, v25\n"
    "v_cvt_pk_bf16_f32 v109, v26, v27\n"
    "ds_write_b64 v206, v[108:109] offset:0\n"
    "v_pk_mul_f32 v[32:33], v[32:33], v[234:235] op_sel_hi:[1,0]\n"
    "v_pk_mul_f32 v[34:35], v[34:35], v[234:235] op_sel_hi:[1,0]\n"
    "v_cvt_pk_bf16_f32 v110, v28, v29\n"
    "v_cvt_pk_bf16_f32 v111, v30, v31\n"
    "ds_write_b64 v207, v[110:111] offset:0\n"
    "v_pk_mul_f32 v[36:37], v[36:37], v[234:235] op_sel_hi:[1,0]\n"
    "v_pk_mul_f32 v[38:39], v[38:39], v[234:235] op_sel_hi:[1,0]\n"
    "v_cvt_pk_bf16_f32 v96, v32, v33\n"
    "v_cvt_pk_bf16_f32 v97, v34, v35\n"
    "ds_write_b64 v200, v[96:97] offset:4096\n"
    "v_pk_mul_f32 v[40:41], v[40:41], v[234:235] op_sel_hi:[1,0]\n"
    "v_pk_mul_f32 v[42:43], v[42:43], v[234:235] op_sel_hi:[1,0]\n"
    "v_cvt_pk_bf16_f32 v98, v36, v37\n"
    "v_cvt_pk_bf16_f32 v99, v38, v39\n"
    "ds_write_b64 v201, v[98:99] offset:4096\n"
    "ds_read_b128 v[160:163], %[srd] offset:0\n"
    "ds_read2_b64 v[164:167], v216 offset0:129 offset1:128\n"
    "v_pk_mul_f32 v[44:45], v[44:45], v[234:235] op_sel_hi:[1,0]\n"
    "v_pk_mul_f32 v[46:47], v[46:47], v[234:235] op_sel_hi:[1,0]\n"
    "v_cvt_pk_bf16_f32 v100, v40, v41\n"
    "v_cvt_pk_bf16_f32 v101, v42, v43\n"
    "ds_write_b64 v202, v[100:101] offset:4096\n"
    "ds_read_b128 v[168:171], %[srd] offset:2048\n"
    "ds_read2_b64 v[172:175], v217 offset0:129 offset1:128\n"
    "v_pk_mul_f32 v[48:49], v[48:49], v[234:235] op_sel_hi:[1,0]\n"
    "v_pk_mul_f32 v[50:51], v[50:51], v[234:235] op_sel_hi:[1,0]\n"
    "v_cvt_pk_bf16_f32 v102, v44, v45\n"
    "v_cvt_pk_bf16_f32 v103, v46, v47\n"
    "s_waitcnt lgkmcnt(11)\n"
    "ds_write_b64 v203, v[102:103] offset:4096\n"
    "v_pk_mul_f32 v[52:53], v[52:53], v[234:235] op_sel_hi:[1,0]\n"
    "v_pk_mul_f32 v[54:55], v[54:55], v[234:235] op_sel_hi:[1,0]\n"
    "v_cvt_pk_bf16_f32 v104, v48, v49\n"
    "v_cvt_pk_bf16_f32 v105, v50, v51\n"
    "ds_write_b64 v204, v[104:105] offset:4096\n"
    "s_waitcnt lgkmcnt(6)\n"
    "buffer_store_dwordx4 v[160:163], %[go], %[ro], s74 offen offset:0 " STPOL "\n"
    "v_pk_mul_f32 v[56:57], v[56:57], v[234:235] op_sel_hi:[1,0]\n"
    "v_pk_mul_f32 v[58:59], v[58:59], v[234:235] op_sel_hi:[1,0]\n"
    "v_cvt_pk_bf16_f32 v106, v52, v53\n"
    "v_cvt_pk_bf16_f32 v107, v54, v55\n"
    "ds_write_b64 v205, v[106:107] offset:4096\n"
    "s_waitcnt lgkmcnt(6)\n"
    "buffer_store_dwordx4 v[164:167], %[go], %[ro], s75 offen offset:0 " STPOL "\n"
    "v_pk_mul_f32 v[60:61], v[60:61], v[234:235] op_sel_hi:[1,0]\n"
    "v_pk_mul_f32 v[62:63], v[62:63], v[234:235] op_sel_hi:[1,0]\n"
    "v_cvt_pk_bf16_f32 v108, v56, v57\n"
    "v_cvt_pk_bf16_f32 v109, v58, v59\n"
    "ds_write_b64 v206, v[108:109] offset:4096\n"
    "s_waitcnt lgkmcnt(5)\n"
    "buffer_store_dwordx4 v[168:171], %[go], %[ro], s71 offen offset:0 " STPOL "\n"
    "v_pk_mul_f32 v[64:65], v[64:65], v[234:235] op_sel_hi:[1,0]\n"
    "v_pk_mul_f32 v[66:67], v[66:67], v[234:235] op_sel_hi:[1,0]\n"
    "v_cvt_pk_bf16_f32 v110, v60, v61\n"
    "v_cvt_pk_bf16_f32 v111, v62, v63\n"
    "ds_write_b64 v207, v[110:111] offset:4096\n"
    "s_waitcnt lgkmcnt(5)\n"
    "buffer_store_dwordx4 v[172:175], %[go], %[ro], s72 offen offset:0 " STPOL "\n"
    "v_pk_mul_f32 v[68:69], v[68:69], v[234:235] op_sel_hi:[1,0]\n"
    "v_pk_mul_f32 v[70:71], v[70:71], v[234:235] op_sel_hi:[1,0]\n"
    "v_cvt_pk_bf16_f32 v96, v64, v65\n"
    "v_cvt_pk_bf16_f32 v97, v66, v67\n"
    "ds_write_b64 v200, v[96:97] offset:8192\n"
    "v_pk_mul_f32 v[72:73], v[72:73], v[234:235] op_sel_hi:[1,0]\n"
    "v_pk_mul_f32 v[74:75], v[74:75], v[234:235] op_sel_hi:[1,0]\n"
    "v_cvt_pk_bf16_f32 v98, v68, v69\n"
    "v_cvt_pk_bf16_f32 v99, v70, v71\n"
    "ds_write_b64 v201, v[98:99] offset:8192\n"
    "ds_read_b128 v[176:179], %[srd] offset:4096\n"
    "ds_read2_b64 v[180:183], v218 offset0:129 offset1:128\n"
    "v_pk_mul_f32 v[76:77], v[76:77], v[234:235] op_sel_hi:[1,0]\n"
    "v_pk_mul_f32 v[78:79], v[78:79], v[234:235] op_sel_hi:[1,0]\n"
    "v_cvt_pk_bf16_f32 v100, v72, v73\n"
    "v_cvt_pk_bf16_f32 v101, v74, v75\n"
    "ds_write_b64 v202, v[100:101] offset:8192\n"
    "ds_read_b128 v[184:187], %[srd] offset:6144\n"
    "ds_read2_b64 v[188:191], v219 offset0:129 offset1:128\n"
    "v_pk_mul_f32 v[80:81], v[80:81], v[234:235] op_sel_hi:[1,0]\n"
    "v_pk_mul_f32 v[82:83], v[82:83], v[234:235] op_sel_hi:[1,0]\n"
    "v_cvt_pk_bf16_f32 v102, v76, v77\n"
    "v_cvt_pk_bf16_f32 v103, v78, v79\n"
    "ds_write_b64 v203, v[102:103] offset:8192\n"
    "v_pk_mul_f32 v[84:85], v[84:85], v[234:235] op_sel_hi:[1,0]\n"
    "v_pk_mul_f32 v[86:87], v[86:87], v[234:235] op_sel_hi:[1,0]\n"
    "v_cvt_pk_bf16_f32 v104, v80, v81\n"
    "v_cvt_pk_bf16_f32 v105, v82, v83\n"
    "ds_write_b64 v204, v[104:105] offset:8192\n"
    "s_waitcnt lgkmcnt(6)\n"
    "buffer_store_dwordx4 v[176:179], %[go], %[ro], s74 offen offset:128 " STPOL "\n"
    "v_pk_mul_f32 v[88:89], v[88:89], v[234:235] op_sel_hi:[1,0]\n"
    "v_pk_mul_f32 v[90:91], v[90:91], v[234:235] op_sel_hi:[1,0]\n"
    "v_cvt_pk_bf16_f32 v106, v84, v85\n"
    "v_cvt_pk_bf16_f32 v107, v86, v87\n"
    "ds_write_b64 v205, v[106:107] offset:8192\n"
    "s_waitcnt lgkmcnt(6)\n"
    "buffer_store_dwordx4 v[180:183], %[go], %[ro], s75 offen offset:128 " STPOL "\n"
    "v_pk_mul_f32 v[92:93], v[92:93], v[234:235] op_sel_hi:[1,0]\n"
    "v_pk_mul_f32 v[94:95], v[94:95], v[234:235] op_sel_hi:[1,0]\n"
    "v_cvt_pk_bf16_f32 v108, v88, v89\n"
    "v_cvt_pk_bf16_f32 v109, v90, v91\n"
    "ds_write_b64 v206, v[108:109] offset:8192\n"
    "s_waitcnt lgkmcnt(5)\n"
    "buffer_store_dwordx4 v[184:187], %[go], %[ro], s71 offen offset:128 " STPOL "\n"
    "v_cvt_pk_bf16_f32 v110, v92, v93\n"
    "v_cvt_pk_bf16_f32 v111, v94, v95\n"
    "ds_write_b64 v207, v[110:111] offset:8192\n"
    "s_waitcnt lgkmcnt(5)\n"
    "buffer_store_dwordx4 v[188:191], %[go], %[ro], s72 offen offset:128 " STPOL "\n"
    "ds_read_b128 v[160:163], %[srd] offset:8192\n"
    "ds_read2_b64 v[164:167], v220 offset0:129 offset1:128\n"
    "ds_read_b128 v[168:171], %[srd] offset:10240\n"
    "ds_read2_b64 v[172:175], v221 offset0:129 offset1:128\n"
    "s_waitcnt lgkmcnt(3)\n"
    "buffer_store_dwordx4 v[160:163], %[go], %[ro], s74 offen offset:256 " STPOL "\n"
    "s_waitcnt lgkmcnt(2)\n"
    "buffer_store_dwordx4 v[164:167], %[go], %[ro], s75 offen offset:256 " STPOL "\n"
    "s_waitcnt lgkmcnt(1)\n"
    "buffer_store_dwordx4 v[168:171], %[go], %[ro], s71 offen offset:256 " STPOL "\n"
    "s_waitcnt lgkmcnt(0)\n"
    "buffer_store_dwordx4 v[172:175], %[go], %[ro], s72 offen offset:256 " STPOL "\n"
    "s_branch .Lend%=\n"
    ".p2align 6, , 60\n"
    ".Lloopd%=:\n"
    "v_mfma_f32_32x32x16_bf16 v[96:111], v[160:163], v[112:115], 0\n"
    "ds_read_b128 v[160:163], v240 offset:4096\n"
    "ds_read_b64_tr_b16 v[176:177], v244 offset:0\n"
    "v_exp_f32 v232, v224\n"
    "v_add_f32 v236, v236, v235\n"
    "v_cvt_pk_bf16_f32 v211, v234, v235\n"
    "s_cmp_gt_i32 s73, 0\n"
    "s_cselect_b32 s74, s71, %[oob]\n"
    "s_cselect_b32 s75, s72, %[dump]\n"
    "v_mfma_f32_32x32x16_bf16 v[96:111], v[164:167], v[116:119], v[96:111]\n"
    "ds_read_b128 v[164:167], v241 offset:4096\n"
    "ds_read_b64_tr_b16 v[178:179], v244 offset:1024\n"
    "s_add_u32 m0, s75, 0\n"
    "s_nop 0\n"
    "buffer_load_dwordx4 %[voff], %[rkv], s74 offen offset:0 lds\n"
    "v_exp_f32 v233, v225\n"
    "v_add_f32 v236, v236, v232\n"
    "s_waitcnt lgkmcnt(4)\n"
    "v_mfma_f32_32x32x16_bf16 v[96:111], v[168:171], v[120:123], v[96:111]\n"
    "ds_read_b128 v[168:171], v242 offset:4096\n"
    "ds_read_b64_tr_b16 v[180:181], v245 offset:0\n"
    "v_exp_f32 v234, v226\n"
    "v_add_f32 v236, v236, v233\n"
    "v_cvt_pk_bf16_f32 v212, v232, v233\n"
    "v_mfma_f32_32x32x16_bf16 v[96:111], v[172:175], v[124:127], v[96:111]\n"
    "ds_read_b128 v[172:175], v243 offset:4096\n"
    "ds_read_b64_tr_b16 v[182:183], v245 offset:1024\n"
    "v_exp_f32 v235, v227\n"
    "v_add_f32 v236, v236, v234\n"
    "s_waitcnt lgkmcnt(5)\n"
    "v_mfma_f32_32x32x16_bf16 v[96:111], v[160:163], v[128:131], v[96:111]\n"
    "ds_read_b128 v[160:163], v240 offset:8192\n"
    "ds_read_b64_tr_b16 v[184:185], v244 offset:4096\n"
    "v_exp_f32 v232, v228\n"
    "v_add_f32 v236, v236, v235\n"
    "v_cvt_pk_bf16_f32 v213, v234, v235\n"
    "v_mfma_f32_32x32x16_bf16 v[96:111], v[164:167], v[132:135], v[96:111]\n"
    "ds_read_b128 v[164:167], v241 offset:8192\n"
    "ds_read_b64_tr_b16 v[186:187], v244 offset:5120\n"
    "v_exp_f32 v233, v229\n"
    "v_add_f32 v236, v236, v232\n"
    "s_waitcnt lgkmcnt(5)\n"
    "v_mfma_f32_32x32x16_bf16 v[96:111], v[168:171], v[136:139], v[96:111]\n"
    "ds_read_b128 v[168:171], v242 offset:8192\n"
    "ds_read_b64_tr_b16 v[188:189], v245 offset:4096\n"
    "v_exp_f32 v234, v230\n"
    "v_add_f32 v236, v236, v233\n"
    "v_cvt_pk_bf16_f32 v214, v232, v233\n"
    "v_mfma_f32_32x32x16_bf16 v[96:111], v[172:175], v[140:143], v[96:111]\n"
    "ds_read_b128 v[172:175], v243 offset:8192\n"
    "ds_read_b64_tr_b16 v[190:191], v245 offset:5120\n"
    "v_exp_f32 v235, v231\n"
    "v_add_f32 v236, v236, v234\n"
    "s_waitcnt lgkmcnt(5)\n"
    "v_mfma_f32_32x32x16_bf16 v[96:111], v[160:163], v[144:147], v[96:111]\n"
    "ds_read_b64_tr_b16 v[192:193], v244 offset:8192\n"
    "ds_read_b64_tr_b16 v[194:195], v244 offset:9216\n"
    "s_add_u32 m0, s75, 3968\n"
    "s_nop 0\n"
    "buffer_load_dwordx4 %[voff], %[rkv], s74 offen offset:128 lds\n"
    "v_add_f32 v236, v236, v235\n"
    "v_cvt_pk_bf16_f32 v215, v234, v235\n"
    "v_mfma_f32_32x32x16_bf16 v[96:111], v[164:167], v[148:151], v[96:111]\n"
    "ds_read_b64_tr_b16 v[196:197], v245 offset:8192\n"
    "ds_read_b64_tr_b16 v[198:199], v245 offset:9216\n"
    "v_cmp_nge_f32 vcc, s65, v236\n"
    "s_waitcnt lgkmcnt(4)\n"
    "v_mfma_f32_32x32x16_bf16 v[96:111], v[168:171], v[152:155], v[96:111]\n"
    "ds_read_b64_tr_b16 v[200:201], v244 offset:2048\n"
    "ds_read_b64_tr_b16 v[202:203], v244 offset:3072\n"
    "s_cbranch_vccnz .Lrare%=\n"
    ".Lbackd%=:\n"
    "v_add_f32 v237, v237, v236\n"
    "v_mfma_f32_32x32x16_bf16 v[96:111], v[172:175], v[156:159], v[96:111]\n"
    "ds_read_b64_tr_b16 v[204:205], v245 offset:2048\n"
    "ds_read_b64_tr_b16 v[206:207], v245 offset:3072\n"
    "s_add_i32 s67, s66, 24576\n"
    "s_cmp_lg_u32 s66, %[wrap]\n"
    "s_cselect_b32 s67, s67, 0\n"
    "v_mfma_f32_32x32x16_bf16 v[0:15], v[176:179], v[208:211], v[0:15]\n"
    "ds_read_b64_tr_b16 v[176:177], v244 offset:6144\n"
    "ds_read_b64_tr_b16 v[178:179], v244 offset:7168\n"
    "s_add_u32 m0, s75, 7936\n"
    "s_nop 0\n"
    "buffer_load_dwordx4 %[voff], %[rkv], s74 offen offset:256 lds\n"
    "v_add_u32 v240, s67, %[kb]\n"
    "v_xor_b32 v241, 32, v240\n"
    "s_waitcnt lgkmcnt(6)\n"
    "v_mfma_f32_32x32x16_bf16 v[16:31], v[180:183], v[208:211], v[16:31]\n"
    "ds_read_b64_tr_b16 v[180:181], v245 offset:6144\n"
    "ds_read_b64_tr_b16 v[182:183], v245 offset:7168\n"
    "v_xor_b32 v242, 64, v240\n"
    "v_xor_b32 v243, 0x60, v240\n"
    "s_add_i32 s68, s68, -1\n"
    "s_add_i32 s73, s73, -1\n"
    "s_add_u32 s71, s71, 0xfffd0000\n"
    "s_add_u32 s72, s72, 24576\n"
    "s_cmp_lt_u32 s72, %[dend]\n"
    "s_cselect_b32 s72, s72, %[dd0]\n"
    "v_mfma_f32_32x32x16_bf16 v[32:47], v[184:187], v[208:211], v[32:47]\n"
    "ds_read_b64_tr_b16 v[184:185], v244 offset:10240\n"
    "ds_read_b64_tr_b16 v[186:187], v244 offset:11264\n"
    "v_fma_f32 v216, v96, s64, -v238\n"
    "v_fma_f32 v217, v97, s64, -v238\n"
    "v_fma_f32 v218, v98, s64, -v238\n"
    "v_mfma_f32_32x32x16_bf16 v[48:63], v[188:191], v[208:211], v[48:63]\n"
    "ds_read_b64_tr_b16 v[188:189], v245 offset:10240\n"
    "ds_read_b64_tr_b16 v[190:191], v245 offset:11264\n"
    "v_fma_f32 v219, v99, s64, -v238\n"
    "v_fma_f32 v220, v100, s64, -v238\n"
    "s_waitcnt lgkmcnt(8)\n"
    "v_mfma_f32_32x32x16_bf16 v[64:79], v[192:195], v[208:211], v[64:79]\n"
    "v_exp_f32 v232, v216\n"
    "v_fma_f32 v221, v101, s64, -v238\n"
    "v_fma_f32 v222, v102, s64, -v238\n"
    "v_add_u32 v244, s66, %[vb]\n"
    "s_mov_b32 s66, s67\n"
    "v_mfma_f32_32x32x16_bf16 v[80:95], v[196:199], v[208:211], v[80:95]\n"
    "v_exp_f32 v233, v217\n"
    "v_fma_f32 v223, v103, s64, -v238\n"
    "v_fma_f32 v224, v104, s64, -v238\n"
    "v_fma_f32 v225, v105, s64, -v238\n"
    "v_xor_b32 v245, 64, v244\n"
    "v_mfma_f32_32x32x16_bf16 v[0:15], v[200:203], v[212:215], v[0:15]\n"
    "s_waitcnt vmcnt(3) lgkmcnt(0)\n"
    "s_barrier\n"
    "v_exp_f32 v234, v218\n"
    "v_add_f32 v236, v232, v233\n"
    "v_cvt_pk_bf16_f32 v208, v232, v233\n"
    "v_mfma_f32_32x32x16_bf16 v[16:31], v[204:207], v[212:215], v[16:31]\n"
    "ds_read_b128 v[160:163], v240 offset:0\n"
    "v_exp_f32 v235, v219\n"
    "v_fma_f32 v226, v106, s64, -v238\n"
    "v_fma_f32 v227, v107, s64, -v238\n"
    "v_add_f32 v236, v236, v234\n"
    "v_mfma_f32_32x32x16_bf16 v[32:47], v[176:179], v[212:215], v[32:47]\n"
    "ds_read_b128 v[164:167], v241 offset:0\n"
    "v_exp_f32 v232, v220\n"
    "v_fma_f32 v228, v108, s64, -v238\n"
    "v_add_f32 v236, v236, v235\n"
    "v_cvt_pk_bf16_f32 v209, v234, v235\n"
    "v_mfma_f32_32x32x16_bf16 v[48:63], v[180:183], v[212:215], v[48:63]\n"
    "ds_read_b128 v[168:171], v242 offset:0\n"
    "ds_read_b128 v[172:175], v243 offset:0\n"
    "v_exp_f32 v233, v221\n"
    "v_fma_f32 v229, v109, s64, -v238\n"
    "v_add_f32 v236, v236, v232\n"
    "v_mfma_f32_32x32x16_bf16 v[64:79], v[184:187], v[212:215], v[64:79]\n"
    "v_exp_f32 v234, v222\n"
    "v_fma_f32 v230, v110, s64, -v238\n"
    "v_add_f32 v236, v236, v233\n"
    "v_cvt_pk_bf16_f32 v210, v232, v233\n"
    "s_cmp_lg_u32 s68, 0\n"
    "v_mfma_f32_32x32x16_bf16 v[80:95], v[188:191], v[212:215], v[80:95]\n"
    "v_exp_f32 v235, v223\n"
    "v_fma_f32 v231, v111, s64, -v238\n"
    "v_add_f32 v236, v236, v234\n"
    "s_waitcnt lgkmcnt(2)\n"
    "s_cbranch_scc1 .Lloopd%=\n"
    "s_branch .Ltail%=\n"
    ".Lrare%=:\n"
    "v_max3_f32 v232, v216, v217, v218\n"
    "v_max3_f32 v233, v219, v220, v221\n"
    "v_max3_f32 v234, v222, v223, v224\n"
    "v_max3_f32 v235, v225, v226, v227\n"
    "v_max3_f32 v239, v228, v229, v230\n"
    "v_max3_f32 v232, v232, v233, v234\n"
    "v_max3_f32 v239, v239, v231, v235\n"
    "v_max_f32 v239, v239, v232\n"
    "v_mov_b32 v232, v239\n"
    "s_nop 1\n"
    "v_permlane32_swap_b32 v239, v232\n"
    "s_nop 1\n"
    "v_max_f32 v239, v239, v232\n"
    "v_max_f32 v239, 0, v239\n"
    "v_exp_f32 v232, -v239\n"
    "v_add_f32 v238, v238, v239\n"
    "v_mul_f32 v237, v237, v232\n"
    "v_pk_mul_f32 v[0:1], v[0:1], v[232:233] op_sel_hi:[1,0]\n"
    "v_pk_mul_f32 v[2:3], v[2:3], v[232:233] op_sel_hi:[1,0]\n"
    "v_pk_mul_f32 v[4:5], v[4:5], v[232:233] op_sel_hi:[1,0]\n"
    "v_pk_mul_f32 v[6:7], v[6:7], v[232:233] op_sel_hi:[1,0]\n"
    "v_pk_mul_f32 v[8:9], v[8:9], v[232:233] op_sel_hi:[1,0]\n"
    "v_pk_mul_f32 v[10:11], v[10:11], v[232:233] op_sel_hi:[1,0]\n"
    "v_pk_mul_f32 v[12:13], v[12:13], v[232:233] op_sel_hi:[1,0]\n"
    "v_pk_mul_f32 v[14:15], v[14:15], v[232:233] op_sel_hi:[1,0]\n"
    "v_pk_mul_f32 v[16:17], v[16:17], v[232:233] op_sel_hi:[1,0]\n"
    "v_pk_mul_f32 v[18:19], v[18:19], v[232:233] op_sel_hi:[1,0]\n"
    "v_pk_mul_f32 v[20:21], v[20:21], v[232:233] op_sel_hi:[1,0]\n"
    "v_pk_mul_f32 v[22:23], v[22:23], v[232:233] op_sel_hi:[1,0]\n"
    "v_pk_mul_f32 v[24:25], v[24:25], v[232:233] op_sel_hi:[1,0]\n"
    "v_pk_mul_f32 v[26:27], v[26:27], v[232:233] op_sel_hi:[1,0]\n"
    "v_pk_mul_f32 v[28:29], v[28:29], v[232:233] op_sel_hi:[1,0]\n"
    "v_pk_mul_f32 v[30:31], v[30:31], v[232:233] op_sel_hi:[1,0]\n"
    "v_pk_mul_f32 v[32:33], v[32:33], v[232:233] op_sel_hi:[1,0]\n"
    "v_pk_mul_f32 v[34:35], v[34:35], v[232:233] op_sel_hi:[1,0]\n"
    "v_pk_mul_f32 v[36:37], v[36:37], v[232:233] op_sel_hi:[1,0]\n"
    "v_pk_mul_f32 v[38:39], v[38:39], v[232:233] op_sel_hi:[1,0]\n"
    "v_pk_mul_f32 v[40:41], v[40:41], v[232:233] op_sel_hi:[1,0]\n"
    "v_pk_mul_f32 v[42:43], v[42:43], v[232:233] op_sel_hi:[1,0]\n"
    "v_pk_mul_f32 v[44:45], v[44:45], v[232:233] op_sel_hi:[1,0]\n"
    "v_pk_mul_f32 v[46:47], v[46:47], v[232:233] op_sel_hi:[1,0]\n"
    "v_pk_mul_f32 v[48:49], v[48:49], v[232:233] op_sel_hi:[1,0]\n"
    "v_pk_mul_f32 v[50:51], v[50:51], v[232:233] op_sel_hi:[1,0]\n"
    "v_pk_mul_f32 v[52:53], v[52:53], v[232:233] op_sel_hi:[1,0]\n"
    "v_pk_mul_f32 v[54:55], v[54:55], v[232:233] op_sel_hi:[1,0]\n"
    "v_pk_mul_f32 v[56:57], v[56:57], v[232:233] op_sel_hi:[1,0]\n"
    "v_pk_mul_f32 v[58:59], v[58:59], v[232:233] op_sel_hi:[1,0]\n"
    "v_pk_mul_f32 v[60:61], v[60:61], v[232:233] op_sel_hi:[1,0]\n"
    "v_pk_mul_f32 v[62:63], v[62:63], v[232:233] op_sel_hi:[1,0]\n"
    "v_pk_mul_f32 v[64:65], v[64:65], v[232:233] op_sel_hi:[1,0]\n"
    "v_pk_mul_f32 v[66:67], v[66:67], v[232:233] op_sel_hi:[1,0]\n"
    "v_pk_mul_f32 v[68:69], v[68:69], v[232:233] op_sel_hi:[1,0]\n"
    "v_pk_mul_f32 v[70:71], v[70:71], v[232:233] op_sel_hi:[1,0]\n"
    "v_pk_mul_f32 v[72:73], v[72:73], v[232:233] op_sel_hi:[1,0]\n"
    "v_pk_mul_f32 v[74:75], v[74:75], v[232:233] op_sel_hi:[1,0]\n"
    "v_pk_mul_f32 v[76:77], v[76:77], v[232:233] op_sel_hi:[1,0]\n"
    "v_pk_mul_f32 v[78:79], v[78:79], v[232:233] op_sel_hi:[1,0]\n"
    "v_pk_mul_f32 v[80:81], v[80:81], v[232:233] op_sel_hi:[1,0]\n"
    "v_pk_mul_f32 v[82:83], v[82:83], v[232:233] op_sel_hi:[1,0]\n"
    "v_pk_mul_f32 v[84:85], v[84:85], v[232:233] op_sel_hi:[1,0]\n"
    "v_pk_mul_f32 v[86:87], v[86:87], v[232:233] op_sel_hi:[1,0]\n"
    "v_pk_mul_f32 v[88:89], v[88:89], v[232:233] op_sel_hi:[1,0]\n"
    "v_pk_mul_f32 v[90:91], v[90:91], v[232:233] op_sel_hi:[1,0]\n"
    "v_pk_mul_f32 v[92:93], v[92:93], v[232:233] op_sel_hi:[1,0]\n"
    "v_pk_mul_f32 v[94:95], v[94:95], v[232:233] op_sel_hi:[1,0]\n"
    "v_sub_f32 v216, v216, v239\n"
    "v_sub_f32 v217, v217, v239\n"
    "v_sub_f32 v218, v218, v239\n"
    "v_sub_f32 v219, v219, v239\n"
    "v_sub_f32 v220, v220, v239\n"
    "v_sub_f32 v221, v221, v239\n"
    "v_sub_f32 v222, v222, v239\n"
    "v_sub_f32 v223, v223, v239\n"
    "v_sub_f32 v224, v224, v239\n"
    "v_sub_f32 v225, v225, v239\n"
    "v_sub_f32 v226, v226, v239\n"
    "v_sub_f32 v227, v227, v239\n"
    "v_sub_f32 v228, v228, v239\n"
    "v_sub_f32 v229, v229, v239\n"
    "v_sub_f32 v230, v230, v239\n"
    "v_sub_f32 v231, v231, v239\n"
    "v_exp_f32 v232, v216\n"
    "v_exp_f32 v233, v217\n"
    "v_exp_f32 v234, v218\n"
    "v_add_f32 v236, v232, v233\n"
    "v_cvt_pk_bf16_f32 v208, v232, v233\n"
    "v_exp_f32 v235, v219\n"
    "v_add_f32 v236, v236, v234\n"
    "v_exp_f32 v232, v220\n"
    "v_add_f32 v236, v236, v235\n"
    "v_cvt_pk_bf16_f32 v209, v234, v235\n"
    "v_exp_f32 v233, v221\n"
    "v_add_f32 v236, v236, v232\n"
    "v_exp_f32 v234, v222\n"
    "v_add_f32 v236, v236, v233\n"
    "v_cvt_pk_bf16_f32 v210, v232, v233\n"
    "v_exp_f32 v235, v223\n"
    "v_add_f32 v236, v236, v234\n"
    "v_exp_f32 v232, v224\n"
    "v_add_f32 v236, v236, v235\n"
    "v_cvt_pk_bf16_f32 v211, v234, v235\n"
    "v_exp_f32 v233, v225\n"
    "v_add_f32 v236, v236, v232\n"
    "v_exp_f32 v234, v226\n"
    "v_add_f32 v236, v236, v233\n"
    "v_cvt_pk_bf16_f32 v212, v232, v233\n"
    "v_exp_f32 v235, v227\n"
    "v_add_f32 v236, v236, v234\n"
    "v_exp_f32 v232, v228\n"
    "v_add_f32 v236, v236, v235\n"
    "v_cvt_pk_bf16_f32 v213, v234, v235\n"
    "v_exp_f32 v233, v229\n"
    "v_add_f32 v236, v236, v232\n"
    "v_exp_f32 v234, v230\n"
    "v_add_f32 v236, v236, v233\n"
    "v_cvt_pk_bf16_f32 v214, v232, v233\n"
    "v_exp_f32 v235, v231\n"
    "v_add_f32 v236, v236, v234\n"
    "s_nop 0\n"
    "v_add_f32 v236, v236, v235\n"
    "v_cvt_pk_bf16_f32 v215, v234, v235\n"
    "s_cmp_eq_u32 s70, 0\n"
    "s_cbranch_scc1 .Lbackl%=\n"
    "s_cmp_eq_u32 s70, 1\n"
    "s_cbranch_scc1 .Lbackd%=\n"
    "s_branch .Lbackt%=\n"
    ".Lva_end:\n"
    ".Lend%=:\n"
    :
    : [kb] "v"(kb + rbase), [vb] "v"(vb + rbase), [voff] "v"(voffD), [sw] "v"(sw + stg), [srd] "v"(srd + stg), [go] "v"(go), [bias] "v"(bias),
      [n] "s"(n), [qlds] "s"(qo), [duo] "s"((nq - 1) >> 7), [rkv] "s"(rsD), [sa] "s"(s0 - (2 - half) * TSTEP), [dd] "s"(dD + (2 - half) * SLOTB),
      [dd0] "s"(dD), [dend] "s"(dD + 6 * SLOTB), [rem] "s"(n - 2 + half), [oob] "s"(OOB), [dump] "s"(lds0 + X_OFF), [ro] "s"(rsO), [so] "s"(sq),
      [wrap] "s"(pair ? 2 * SLOTB : 5 * SLOTB)
    : "memory", "vcc", "scc", "v0", "v1", "v2", "v3", "v4", "v5", "v6", "v7", "v8", "v9", "v10", "v11", "v12", "v13", "v14", "v15", "v16", "v17", "v18", "v19", "v20", "v21", "v22", "v23", "v24", "v25", "v26", "v27", "v28", "v29", "v30", "v31", "v32", "v33", "v34", "v35", "v36", "v37", "v38", "v39", "v40", "v41", "v42", "v43", "v44", "v45", "v46", "v47", "v48", "v49", "v50", "v51", "v52", "v53", "v54", "v55", "v56", "v57", "v58", "v59", "v60", "v61", "v62", "v63", "v64", "v65", "v66", "v67", "v68", "v69", "v70", "v71", "v72", "v73", "v74", "v75", "v76", "v77", "v78", "v79", "v80", "v81", "v82", "v83", "v84", "v85", "v86", "v87", "v88", "v89", "v90", "v91", "v92", "v93", "v94", "v95", "v96", "v97", "v98", "v99", "v100", "v101", "v102", "v103", "v104", "v105", "v106", "v107", "v108", "v109", "v110", "v111", "v112", "v113", "v114", "v115", "v116", "v117", "v118", "v119", "v120", "v121", "v122", "v123", "v124", "v125", "v126", "v127", "v128", "v129", "v130", "v131", "v132", "v133", "v134", "v135", "v136", "v137", "v138", "v139", "v140", "v141", "v142", "v143", "v144", "v145", "v146", "v147", "v148", "v149", "v150", "v151", "v152", "v153", "v154", "v155", "v156", "v157", "v158", "v159", "v160", "v161", "v162", "v163", "v164", "v165", "v166", "v167", "v168", "v169", "v170", "v171", "v172", "v173", "v174", "v175", "v176", "v177", "v178", "v179", "v180", "v181", "v182", "v183", "v184", "v185", "v186", "v187", "v188", "v189", "v190", "v191", "v192", "v193", "v194", "v195", "v196", "v197", "v198", "v199", "v200", "v201", "v202", "v203", "v204", "v205", "v206", "v207", "v208", "v209", "v210", "v211", "v212", "v213", "v214", "v215", "v216", "v217", "v218", "v219", "v220", "v221", "v222", "v223", "v224", "v225", "v226", "v227", "v228", "v229", "v230", "v231", "v232", "v233", "v234", "v235", "v236", "v237", "v238", "v239", "v240", "v241", "v242", "v243", "v244", "v245", "s64", "s65", "s66", "s67", "s68", "s69", "s70", "s71", "s72", "s73", "s74", "s75");
  } else if (duo) {
    WAIT_BAR(3); WAIT_BAR(0);
    unsigned slot = (2 - half) * SLOTB;
#pragma nounroll
    for (int t = 2 - half; t < n + 2 - half; ++t) {
      asm volatile("" : "+s"(t));
      if (t < n) DMA3(rsD, voffD, s0 - t * TSTEP, dD + slot); else NULL3(rsD, voffD, sink);
      slot = slot == 5 * SLOTB ? 0 : slot + SLOTB;
      if (t > 2 - half) WAIT_BAR(3);
    }
  } else if (pair) {
    unsigned slot = 0;
#pragma nounroll
    for (int t = 0; t < n + 1; ++t) {
      asm volatile("" : "+s"(t));
      const unsigned nx = slot == 2 * SLOTB ? 0 : slot + SLOTB;
      if (t + 1 < n) { DMA3(rsK, voffK, s0 - (t + 1) * TSTEP, dK + nx); DMA3(rsK, voffK + o16, s0 - (t + 1) * TSTEP, (dK ^ 2048) + nx); } else { NULL3(rsK, voffK, sink); NULL3(rsK, voffK, sink); }
      if (t < n) { DMA3(rsV, voffV, s0 - t * TSTEP, dV + slot); DMA3(rsV, voffV + o16, s0 - t * TSTEP, (dV ^ 2048) + slot); } else { NULL3(rsV, voffV, sink); NULL3(rsV, voffV, sink); }
      slot = nx;
      if (t == 0) { WAIT_BAR(12); WAIT_BAR(0); }
      if (t >= 2) WAIT_BAR(12);
    }
  } else {
    unsigned slot = 0, so = s0; int left = n;
#define STEP(GAP) do { const unsigned nx = slot == 5 * SLOTB ? 0 : slot + SLOTB; \
      DMA3S(GAP, rsK, voffK, left > 1 ? so - TSTEP : OOB, left > 1 ? dK + nx : sink, left > 1 ? 3968u : -128u); \
      DMA3S(GAP, rsV, voffV, left > 0 ? so : OOB, left > 0 ? dV + slot : sink, left > 0 ? 3968u : -128u); \
      slot = nx; so -= TSTEP; --left; } while (0)
#pragma nounroll
    for (int i = min(RAMP, n); i >= 0; --i) { asm volatile("" : "+s"(left), "+s"(slot)); STEP("s_nop 0\n\t"); WAIT_BAR(6); }
    if (n > RAMP) {
      STEP("s_nop 0\n\t");
#pragma nounroll
      for (int i = n - RAMP; i > 0; --i) { STEP("s_sleep 1\n\t"); WAIT_BAR(12); }
    }
  }
}

extern "C" void kernel_launch(void* const* d_in, const int* in_sizes, int n_in,
                              void* d_out, int out_size, void* d_ws, size_t ws_size,
                              hipStream_t stream) {
  const int T = in_sizes[0] / (NH * HD), nseq = in_sizes[3] - 1;
  (void)hipFuncSetAttribute((const void*)varlen_attn_kernel, hipFuncAttributeMaxDynamicSharedMemorySize, (int)LDS_BYTES);
  for (int s0 = 0; s0 < nseq; s0 += 8) {
    const int ns = nseq - s0 < 8 ? nseq - s0 : 8;
    int G = T / 256 + ns;
    if (G < 16) G = 16;
    hipLaunchKernelGGL(varlen_attn_kernel, dim3(NH * G), dim3(512), LDS_BYTES, stream,
                       (const __hip_bfloat16*)d_in[0], (const __hip_bfloat16*)d_in[1], (const __hip_bfloat16*)d_in[2],
                       (const int*)d_in[3] + s0, (__hip_bfloat16*)d_out, ns, G, T, (unsigned)((T + 2 * G - 9) / (2 * G - 8)));
  }
}
```
